```python
import torch

def _softmax(x, axis=-1):
    return torch.softmax(x, dim=axis)
def _gelu(x):
    return torch.nn.functional.gelu(x, approximate="tanh")
def _silu(x):
    return torch.nn.functional.silu(x)
def _sigmoid(x):
    return torch.sigmoid(x)
def _layernorm(x, axis=-1, eps=1e-5):
    m = x.mean(dim=axis, keepdim=True)
    v = x.var(dim=axis, keepdim=True, unbiased=False)
    return (x - m) / torch.sqrt(v + eps)
def _scatter_add(tgt, idx, src, axis=0):
    return tgt.clone().index_add_(axis, idx, src)
def _segment_sum(src, seg, n):
    out = torch.zeros((n,) + src.shape[1:], dtype=src.dtype)
    return out.index_add_(0, seg, src)
def _pad(x, pw):
    flat = tuple(v for pair in reversed(pw) for v in pair)
    return torch.nn.functional.pad(x, flat)

kernel_name = 'recurrent_scan__ssm_conv_s4__glu_gated_glu__prompt'

def setup_inputs():

    _w = (lambda a: (lambda t: t.to(torch.bfloat16) if t.is_floating_point() else t)(torch.as_tensor(a)))
    def _A_setup():
        B, L, D, N = 1, 2048, 512, 128
        A_re = -(lambda _x: torch.exp(torch.as_tensor(_x)))((lambda *s: torch.randn(*s, dtype=torch.bfloat16) * 6.4)(D, N))
        A_im = torch.pi * (lambda *s: torch.randn(*s, dtype=torch.bfloat16) * 0.05)(D, N)
        B_re = (lambda *s: torch.randn(*s, dtype=torch.bfloat16) * 0.05)(D, N)
        B_im = (lambda *s: torch.randn(*s, dtype=torch.bfloat16) * 0.05)(D, N)
        C_re = (lambda *s: torch.randn(*s, dtype=torch.bfloat16) * 0.05)(D, N)
        C_im = (lambda *s: torch.randn(*s, dtype=torch.bfloat16) * 0.05)(D, N)
        log_dt = (lambda *s: torch.randn(*s, dtype=torch.bfloat16) * 0.05)(D) * 0.5 - 4.0
        Dskip = (lambda *s: torch.randn(*s, dtype=torch.bfloat16) * 0.05)(D)
        return {
            "u": _w((lambda *s: torch.randn(*s, dtype=torch.bfloat16) * 0.05)(B, L, D)),
            "A_re": _w(A_re),
            "A_im": _w(A_im),
            "B_re": _w(B_re),
            "B_im": _w(B_im),
            "C_re": _w(C_re),
            "C_im": _w(C_im),
            "log_dt": _w(log_dt),
            "Dskip": _w(Dskip),
        }
    _A_in = _A_setup()

    def _B_setup():
        B, S, D, F = 1, 2048, 512, 1792
        return {
            "x": _w((lambda *s: torch.randn(*s, dtype=torch.bfloat16) * 0.05)(B, S, D)),
            "w_gate": _w((lambda *s: torch.randn(*s, dtype=torch.bfloat16) * 0.05)(D, F)),
            "w_up": _w((lambda *s: torch.randn(*s, dtype=torch.bfloat16) * 0.05)(D, F)),
            "w_down": _w((lambda *s: torch.randn(*s, dtype=torch.bfloat16) * 0.05)(F, D)),
        }
    _B_in = _B_setup()

    return {
        "A_u": _A_in["u"],
        "A_A_re": _A_in["A_re"],
        "A_A_im": _A_in["A_im"],
        "A_B_re": _A_in["B_re"],
        "A_B_im": _A_in["B_im"],
        "A_C_re": _A_in["C_re"],
        "A_C_im": _A_in["C_im"],
        "A_log_dt": _A_in["log_dt"],
        "A_Dskip": _A_in["Dskip"],
        "B_x": _B_in["x"],
        "B_w_gate": _B_in["w_gate"],
        "B_w_up": _B_in["w_up"],
        "B_w_down": _B_in["w_down"],
    }

def reference(A_u, A_A_re, A_A_im, A_B_re, A_B_im, A_C_re, A_C_im, A_log_dt, A_Dskip, B_x, B_w_gate, B_w_up, B_w_down):
    def _A_ref(u, A_re, A_im, B_re, B_im, C_re, C_im, log_dt, Dskip):
        Bsz, L, D = u.shape
        dt = torch.exp(log_dt)[:, None]
        A = A_re + 1j * A_im
        Bc = B_re + 1j * B_im
        Cc = C_re + 1j * C_im
        logdA = dt * A
        dB = dt * Bc
        l = (lambda x, dt: x.to(dt))(torch.arange(L), A_re.dtype)
        P = torch.exp(logdA[:, :, None] * l[None, None, :])
        K = torch.einsum("dn,dnl,dn->dl", Cc, P, dB).real
        L2 = 2 * L
        Kp = _pad(K, ((0, 0), (0, L)))
        up = (lambda x, *d: x.permute(*d))(u, 0, 2, 1)
        up = _pad(up, ((0, 0), (0, 0), (0, L)))
        Kf = (lambda x, axes=(-2,-1): torch.fft.rfft2(x.float(), dim=axes))(Kp[:, None, :], axes=(-2, -1))
        Uf = (lambda x, axes=(-2,-1): torch.fft.rfft2(x.float(), dim=axes))(up[:, :, None, :], axes=(-2, -1))
        Yf = Uf * Kf[None, :, :, :]
        y = (lambda x, s, axes=(-2,-1): torch.fft.irfft2(x, s=s, dim=axes).to(torch.bfloat16))(Yf, (1, L2), axes=(-2, -1))[:, :, 0, :L]
        y = (lambda x, *d: x.permute(*d))(y, 0, 2, 1) + u * Dskip[None, None, :]
        return y

    def _B_ref(x, w_gate, w_up, w_down):
        g = torch.einsum("bsd,df->bsf", x, w_gate)
        u = torch.einsum("bsd,df->bsf", x, w_up)
        h = _sigmoid(g) * u
        return torch.einsum("bsf,fd->bsd", h, w_down)

    x = A_u
    xn = _layernorm(x, axis=-1)
    s = _A_ref(xn, A_A_re, A_A_im, A_B_re, A_B_im, A_C_re, A_C_im, A_log_dt, A_Dskip)
    g = _silu(_B_ref(xn, B_w_gate, B_w_up, B_w_down))
    return x + s * g
```

```cpp
#include <hip/hip_runtime.h>
#include <hip/hip_bf16.h>
#include <stdint.h>
#define ROUND_L 1
#define MIMIC 1
typedef unsigned short u16;
__device__ __forceinline__ float bf2f(u16 v){ return __uint_as_float(((unsigned)v)<<16); }
__device__ __forceinline__ u16 f2bf(float f){ __hip_bfloat16 b = __float2bfloat16(f); return *reinterpret_cast<u16*>(&b); }
__device__ __forceinline__ float rb(float f){
  return bf2f(f2bf(f));
}
constexpr int L=2048, D=512, N=128, F=1792;

__global__ void k_ln(const u16* x, float* xn){
  int t = blockIdx.x; int lane = threadIdx.x;
  float v[8]; float s=0;
  for(int i=0;i<8;i++){ v[i]=bf2f(x[t*D+lane*8+i]); s+=v[i]; }
  for(int o=32;o;o>>=1) s+=__shfl_xor(s,o);
  float mean=s/D;
  float q=0; for(int i=0;i<8;i++){ float d=v[i]-mean; q+=d*d; }
  for(int o=32;o;o>>=1) q+=__shfl_xor(q,o);
  float var=rb(q/D); float m=rb(mean);
  float den=rb(sqrtf(rb(var+1e-5f)));
  for(int i=0;i<8;i++){ xn[t*D+lane*8+i]=rb(rb(v[i]-m)/den); }
}
__global__ void k_kgen(const u16* Are,const u16* Aim,const u16* Bre,const u16* Bim,const u16* Cre,const u16* Cim,const u16* logdt,float* K){
  int d=blockIdx.y; int l=blockIdx.x*256+threadIdx.x;
  float dt=rb(expf(bf2f(logdt[d])));
  float lf=bf2f(f2bf((float)l));
  double acc=0;
  for(int n=0;n<N;n++){
    float ar=dt*bf2f(Are[d*N+n]), ai=dt*bf2f(Aim[d*N+n]);
    float br=dt*bf2f(Bre[d*N+n]), bi=dt*bf2f(Bim[d*N+n]);
    float cr=bf2f(Cre[d*N+n]), ci=bf2f(Cim[d*N+n]);
    float wr=cr*br-ci*bi, wi=cr*bi+ci*br;
    float e=expf(ar*lf); float sn,cs; sincosf(ai*lf,&sn,&cs);
    acc += (double)(e*(wr*cs-wi*sn));
  }
  K[d*L+l]=(float)acc;
}
__global__ void k_conv(const float* xn,const float* K,const u16* Dskip,float* s){
  int t=blockIdx.x; int d=blockIdx.y*256+threadIdx.x;
  float acc=0;
  for(int l=0;l<=t;l++) acc += K[d*L+l]*xn[(t-l)*D+d];
  float y=rb(acc);
  s[t*D+d]=rb(y+rb(xn[t*D+d]*bf2f(Dskip[d])));
}
__global__ void k_g1(const float* xn,const u16* wg,const u16* wu,float* h){
  int t=blockIdx.x; int f=blockIdx.y*256+threadIdx.x;
  __shared__ float xs[D];
  for(int i=threadIdx.x;i<D;i+=256) xs[i]=xn[t*D+i];
  __syncthreads();
  float g=0,u=0;
  for(int k=0;k<D;k++){ g+=xs[k]*bf2f(wg[k*F+f]); u+=xs[k]*bf2f(wu[k*F+f]); }
  g=rb(g); u=rb(u);
  float sg=rb(1.f/(1.f+expf(-g)));
  h[t*F+f]=rb(sg*u);
}
__global__ void k_g2(const float* h,const u16* wd,const float* s,const u16* x,u16* out){
  int t=blockIdx.x; int d=blockIdx.y*256+threadIdx.x;
  __shared__ float hs[F];
  for(int i=threadIdx.x;i<F;i+=256) hs[i]=h[t*F+i];
  __syncthreads();
  float a=0;
  for(int k=0;k<F;k++) a+=hs[k]*bf2f(wd[k*D+d]);
  a=rb(a);
  float g=rb(a/(1.f+expf(-a)));
  out[t*D+d]=f2bf(bf2f(x[t*D+d])+rb(s[t*D+d]*g));
}
extern "C" void kernel_launch(void* const* d_in, const int* in_sizes, int n_in, void* d_out, int out_size, void* d_ws, size_t ws_size, hipStream_t stream){
  const u16* x=(const u16*)d_in[0];
  float* ws=(float*)d_ws;
  float* xn=ws; float* K=xn+L*D; float* s=K+D*L; float* h=s+L*D;
  k_ln<<<L,64,0,stream>>>(x,xn);
  k_kgen<<<dim3(L/256,D),256,0,stream>>>((const u16*)d_in[1],(const u16*)d_in[2],(const u16*)d_in[3],(const u16*)d_in[4],(const u16*)d_in[5],(const u16*)d_in[6],(const u16*)d_in[7],K);
  k_conv<<<dim3(L,2),256,0,stream>>>(xn,K,(const u16*)d_in[8],s);
  k_g1<<<dim3(L,F/256),256,0,stream>>>(xn,(const u16*)d_in[10],(const u16*)d_in[11],h);
  k_g2<<<dim3(L,2),256,0,stream>>>(h,(const u16*)d_in[12],s,x,(u16*)d_out);
}
```

```cpp
#include <hip/hip_runtime.h>
#include <stdint.h>
#include <type_traits>

typedef unsigned short u16;
typedef unsigned int u32;
typedef unsigned long long u64;
typedef __attribute__((ext_vector_type(2))) float f32x2;
typedef __attribute__((ext_vector_type(4))) float f32x4;
typedef __attribute__((ext_vector_type(2))) u32 u32x2;
typedef __attribute__((ext_vector_type(4))) u32 u32x4;
typedef __attribute__((ext_vector_type(4))) short s16x4;
typedef __attribute__((ext_vector_type(8))) __bf16 bf16x8;
typedef __attribute__((ext_vector_type(2))) __bf16 bf16x2;
typedef __attribute__((ext_vector_type(8))) _Float16 f16x8;
typedef __attribute__((ext_vector_type(2))) _Float16 f16x2;
#define LAS __attribute__((address_space(3)))
#define GAS __attribute__((address_space(1)))
#define DEV __device__ __forceinline__
typedef __amdgpu_buffer_rsrc_t rsrc_t;

constexpr int L = 2048, D = 512, NM = 128, F = 1792;
constexpr int NTHR = 512;
constexpr int LDS_BYTES = 163840;
constexpr int SC1 = 16;
constexpr int CODE_KB = 40;

constexpr int WS_EPOCH = 0;
constexpr int WS_FA = 4096;
constexpr int WS_FW = 8192;
constexpr int WS_F5 = 73728;
constexpr int WS_F6 = 102400;
constexpr int WS_STATS = 1 << 18;
constexpr int WS_CSUM = WS_STATS + 32768;
constexpr int WS_XT = 1 << 20;
constexpr int WS_WDT = (3 << 20) + 4096;
constexpr int WS_ST = 5 << 20;
constexpr int WS_HT = 7 << 20;
constexpr int WS_END = 15 << 20;
struct GTab {
  u32 v[2048];
  constexpr GTab() : v() {
    for (int i = 0; i < 2048; i++) {
      const int lag = 2047 - i;
      int sh = 0;
      while ((lag >> sh) >= 256) sh++;
      int m = lag >> sh;
      const int rem = lag - (m << sh), half = sh ? 1 << (sh - 1) : 0;
      if (sh && (rem > half || (rem == half && (m & 1)))) m++;
      v[i] = (u32)(m + 128 * sh) * 4;
    }
  }
};
__device__ const GTab g_tab = GTab();

struct Params {
  const u16* x; const u16* are; const u16* aim; const u16* bre; const u16* bim; const u16* cre; const u16* cim;
  const u16* logdt; const u16* dskip; const u16* wg; const u16* wu; const u16* wd;
  u16* out; char* ws;
};

DEV float bf2f(u32 v) { return __uint_as_float(v << 16); }
DEV float bflo(u32 v) { return __uint_as_float(v << 16); }
DEV float bfhi(u32 v) { return __uint_as_float(v & 0xffff0000u); }
DEV u32 pkbf(float a, float b) { f32x2 p = {a, b}; return __builtin_bit_cast(u32, __builtin_convertvector(p, bf16x2)); }
DEV u32 pkh(float a, float b) { f32x2 p = {a, b}; return __builtin_bit_cast(u32, __builtin_convertvector(p, f16x2)); }
DEV float rbf(float a) { return bfhi(pkbf(a, a)); }
DEV float h2f_lo(u32 v) { f16x2 h = __builtin_bit_cast(f16x2, v); return (float)h[0]; }
DEV float h2f_hi(u32 v) { f16x2 h = __builtin_bit_cast(f16x2, v); return (float)h[1]; }

DEV f32x4 mfma_bf(u32x4 a, u32x4 b, f32x4 c) {
  return __builtin_amdgcn_mfma_f32_16x16x32_bf16(__builtin_bit_cast(bf16x8, a), __builtin_bit_cast(bf16x8, b), c, 0, 0, 0);
}
DEV f32x4 mfma_h(u32x4 a, u32x4 b, f32x4 c) {
  return __builtin_amdgcn_mfma_f32_16x16x32_f16(__builtin_bit_cast(f16x8, a), __builtin_bit_cast(f16x8, b), c, 0, 0, 0);
}
template <bool FIRST> DEV void mma_bf(f32x4& c, u32x4 a, u32x4 b) {
  if constexpr (FIRST) asm volatile("v_mfma_f32_16x16x32_bf16 %0, %1, %2, 0" : "=&v"(c) : "v"(a), "v"(b));
  else asm volatile("v_mfma_f32_16x16x32_bf16 %0, %1, %2, %0" : "+v"(c) : "v"(a), "v"(b));
}
template <bool FIRST> DEV void mma_h(f32x4& c, u32x4 a, u32x4 b) {
  if constexpr (FIRST) asm volatile("v_mfma_f32_16x16x32_f16 %0, %1, %2, 0" : "=&v"(c) : "v"(a), "v"(b));
  else asm volatile("v_mfma_f32_16x16x32_f16 %0, %1, %2, %0" : "+v"(c) : "v"(a), "v"(b));
}
#define MFMA_DONE() do { __builtin_amdgcn_sched_barrier(0); asm volatile("s_nop 15\n\ts_nop 7"); __builtin_amdgcn_sched_barrier(0); } while (0)
DEV u32x4 lds_r128(LAS char* lds, u32 off) { return *(LAS u32x4*)(lds + off); }
DEV u32x2 lds_r64(LAS char* lds, u32 off) { return *(LAS u32x2*)(lds + off); }
DEV void lds_w128(LAS char* lds, u32 off, u32x4 v) { *(LAS u32x4*)(lds + off) = v; }
DEV void lds_w64(LAS char* lds, u32 off, u32x2 v) { *(LAS u32x2*)(lds + off) = v; }
DEV void lds_w32(LAS char* lds, u32 off, u32 v) { *(LAS u32*)(lds + off) = v; }
DEV u32 lds_r32(LAS char* lds, u32 off) { return *(LAS u32*)(lds + off); }
DEV float lds_rf(LAS char* lds, u32 off) { return *(LAS float*)(lds + off); }
DEV u32x2 lds_tr(LAS char* lds, u32 off) {
  return __builtin_bit_cast(u32x2, __builtin_amdgcn_ds_read_tr16_b64_v4i16((LAS s16x4*)(lds + off)));
}
DEV u32x4 cat(u32x2 a, u32x2 b) { u32x4 r = {a[0], a[1], b[0], b[1]}; return r; }

#define BAR() asm volatile("s_waitcnt lgkmcnt(0)\n\ts_barrier" ::: "memory")
#define WAITV(n) do { asm volatile("s_waitcnt vmcnt(" #n ")" ::: "memory"); __builtin_amdgcn_s_waitcnt(((n) & 15) | (((n) >> 4) << 14) | 0x0f70); } while (0)
#define SB() __builtin_amdgcn_sched_barrier(0)
#define PIN(x) asm volatile("" : "+v"(x))
#define MEMORD() asm volatile("" ::: "memory")
#define FRESH_LANE(x) asm volatile("v_mbcnt_lo_u32_b32 %0, -1, 0\n\tv_mbcnt_hi_u32_b32 %0, -1, %0" : "=v"(x))

template <int AUX>
DEV void glds16(u32 voff, const void* sbase, u32 ldsbase) {
  u32 keep;
  if constexpr (AUX == 0)
    asm volatile("s_mov_b32 %0, m0\n\ts_mov_b32 m0, %3\n\ts_nop 2\n\tglobal_load_lds_dwordx4 %1, %2\n\ts_mov_b32 m0, %0"
                 : "=&s"(keep) : "v"(voff), "s"(sbase), "s"(ldsbase) : "memory");
  else
    asm volatile("s_mov_b32 %0, m0\n\ts_mov_b32 m0, %3\n\ts_nop 2\n\tglobal_load_lds_dwordx4 %1, %2 sc1\n\ts_mov_b32 m0, %0"
                 : "=&s"(keep) : "v"(voff), "s"(sbase), "s"(ldsbase) : "memory");
}

DEV u32x4 ws_ld128(rsrc_t r, int off) { return __builtin_amdgcn_raw_buffer_load_b128(r, off, 0, SC1); }
DEV u32x4 ws_ld128s(rsrc_t r, int voff, int soff) { return __builtin_amdgcn_raw_buffer_load_b128(r, voff, soff, SC1); }
DEV u32x2 ws_ld64(rsrc_t r, int off) { return __builtin_amdgcn_raw_buffer_load_b64(r, off, 0, SC1); }
DEV void ws_st128(rsrc_t r, int off, u32x4 v) { __builtin_amdgcn_raw_buffer_store_b128(v, r, off, 0, SC1); }
DEV void ws_st128s(rsrc_t r, int voff, int soff, u32x4 v) { __builtin_amdgcn_raw_buffer_store_b128(v, r, voff, soff, SC1); asm volatile("s_nop 1" :: "v"(v) : "memory"); }
DEV void ws_st64(rsrc_t r, int off, u32x2 v) { __builtin_amdgcn_raw_buffer_store_b64(v, r, off, 0, SC1); }
DEV void ws_st32(rsrc_t r, int off, u32 v) { __builtin_amdgcn_raw_buffer_store_b32(v, r, off, 0, SC1); }

template <int CTRL> DEV float dppf(float v) {
  return __builtin_bit_cast(float, __builtin_amdgcn_update_dpp(0, __builtin_bit_cast(int, v), CTRL, 0xf, 0xf, true));
}
DEV float sum8(float v) { v += dppf<0xB1>(v); v += dppf<0x4E>(v); v += dppf<0x141>(v); return v; }

DEV void set_flag(char* ws, int off, u64 tag) { __hip_atomic_store((GAS u64*)(ws + off), tag, __ATOMIC_RELAXED, __HIP_MEMORY_SCOPE_AGENT); }
DEV u64 get_flag(char* ws, int off) { return __hip_atomic_load((GAS u64*)(ws + off), __ATOMIC_RELAXED, __HIP_MEMORY_SCOPE_AGENT); }
DEV void poll_async(u64& v, char* ws, u32 off) { asm volatile("s_nop 4\n\tglobal_load_dwordx2 %0, %1, %2 sc1" : "+v"(v) : "v"(off), "s"(ws) : "memory"); }
DEV u32 bperm(u32 srcl4, u32 v) { return (u32)__builtin_amdgcn_ds_bpermute((int)srcl4, (int)v); }
DEV void spin_flags(char* ws, int off, u64 tag) {
#pragma unroll 1
  for (u32 spins = 0; spins < (1u << 16); spins++) {
    if (__all(get_flag(ws, off) == tag)) break;
    __builtin_amdgcn_s_sleep(4);
  }
}
DEV void wait_flags(char* ws, int off, u64 first, u64 tag) {
  if (!__all(first == tag)) spin_flags(ws, off, tag);
  __builtin_amdgcn_fence(__ATOMIC_ACQUIRE, "wavefront");
  MEMORD();
}

constexpr u32 SLOT = 49152;
constexpr u32 TW_OFF = 3 * SLOT;
constexpr u32 KG_OFF = 2 * SLOT;
constexpr u32 KG_P = 528, KG_T = 16 * KG_P, KG_CH = 62 * KG_P;
constexpr u32 KD_OFF = 0;
constexpr u32 U_PITCH = 4160, U_CH = 4 * U_PITCH, U_OFF = 6144;
constexpr u32 KR_OFF = 3 * SLOT, KR_CH = 5120;
constexpr u32 RDY_OFF = KR_OFF + 2 * KR_CH + 2048;
constexpr u32 HS = 32768;
constexpr u32 ST_OFF = 3 * SLOT;
constexpr u32 R2_OFF = SLOT;
constexpr u32 hslot(int j) { return (j & 3) == 3 ? U_OFF : SLOT + (j & 3) * HS; }
DEV u32 cr_off(int w) { return w < 3 ? w * 2048 : w < 7 ? U_OFF + 2 * U_CH + (w - 3) * 2048 : KR_OFF + 2 * KR_CH; }
static_assert(U_OFF + 2 * U_CH + 4 * 2048 <= SLOT && KR_OFF + 2 * KR_CH + 2048 <= LDS_BYTES, "lds map");

DEV float fmul(float a, float b) { float o; asm("v_mul_f32 %0, %1, %2" : "=v"(o) : "v"(a), "v"(b)); return o; }
DEV float fadd(float a, float b) { float o; asm("v_add_f32 %0, %1, %2" : "=v"(o) : "v"(a), "v"(b)); return o; }
DEV float ffma(float a, float b, float c) { float o; asm("v_fma_f32 %0, %1, %2, %3" : "=v"(o) : "v"(a), "v"(b), "v"(c)); return o; }
DEV float fnma(float a, float b, float c) { float o; asm("v_fma_f32 %0, -%1, %2, %3" : "=v"(o) : "v"(a), "v"(b), "v"(c)); return o; }
DEV float fmuls(float v, float k) { float o; asm("v_mul_f32 %0, %1, %2" : "=v"(o) : "s"(k), "v"(v)); return o; }
DEV float ffmas(float v, float k, float c) { float o; asm("v_fma_f32 %0, %1, %2, %3" : "=v"(o) : "v"(v), "s"(k), "v"(c)); return o; }
struct cf { float r, i; };
constexpr int NWD0 = 8, NWE = 8, NWP = 2, NWG = 2;
struct KParams { u32 are, aim, bre, bim, cre, cim, ldt; };

struct G1Stage { u32 vw, vx; const char* wg; const char* wu; const char* x; int w, rot; };
template <int I>
DEV void g1_piece(const G1Stage& st, int s0, u32 slotbase) {
  const int s = (s0 + st.rot) & 7;
  if constexpr (I < 4) {
    const size_t wrow = (size_t)(64 * s + 4 * st.w + 32 * (I & 1)) * (F * 2);
    glds16<0>(st.vw, (I < 2 ? st.wg : st.wu) + wrow, slotbase + (I >> 1) * 16384 + (st.w + 8 * (I & 1)) * 1024);
  } else {
    const size_t xo = (size_t)(8 * st.w + 64 * (I & 1)) * (D * 2) + s * 128;
    glds16<0>(st.vx, st.x + xo, slotbase + 32768 + (st.w + 8 * (I & 1)) * 1024);
  }
}
DEV void g1_issue(const G1Stage& st, int s) {
  const u32 sb = s * SLOT;
  g1_piece<4>(st, s, sb); g1_piece<0>(st, s, sb); g1_piece<2>(st, s, sb); g1_piece<5>(st, s, sb); g1_piece<1>(st, s, sb); g1_piece<3>(st, s, sb);
}
struct G1Frag { u32x4 x[4]; u32x2 w[8]; };
struct G1Addr { u32 x0, x1, w0, w1; };
template <int I>
DEV void g1_rd(G1Frag& f, LAS char* lds, const G1Addr& a, int ks) {
  constexpr int XI[12] = {0, -1, -1, 1, 2, 3, -1, -1, -1, -1, -1, -1};
  constexpr int WI[12] = {-1, 0, 1, -1, -1, -1, 2, 3, 4, 5, 6, 7};
  if constexpr (XI[I] >= 0) f.x[XI[I]] = lds_r128(lds, (ks ? a.x1 : a.x0) + XI[I] * 2048);
  else {
    constexpr int i = WI[I];
    f.w[i] = lds_tr(lds, (((i >> 1) & 1) ? a.w1 : a.w0) + (i >> 2) * 16384 + ks * 8192 + (i & 1) * 1024);
  }
}
template <int C, int I, class H>
DEV void g1_step(f32x4 (&acc)[4][4], const G1Frag& cur, G1Frag& nxt, LAS char* lds, const G1Addr& a, H& hk) {
  mma_bf<C == 0>(acc[I >> 2][I & 3], cat(cur.w[2 * (I >> 2)], cur.w[2 * (I >> 2) + 1]), cur.x[I & 3]);
  if constexpr (C < 15 && I < 12) g1_rd<I>(nxt, lds, a, (C & 1) ^ 1);
  hk.template at<C, I>();
  SB();
  if constexpr (I < 15) g1_step<C, I + 1>(acc, cur, nxt, lds, a, hk);
}

struct Mid {
  G1Stage st; rsrc_t ws; char* wsp; LAS char* lds; u64 tag;
  int w, xs, cs, es, wds, hs, f5o, h5s, tws, f2s;
  bool helper;
  u32 l16;
  u32x4 e0; u32x2 e1x;
  f32x4 csg[2], csu[2]; u32x2 stt[4];
  f32x4 acc[4][4];
  u32x4 wdf[28];
  u32x4 hq; u64 fv; int ready; const char* hbase; u32 hdst;
  cf w128, w4, e1, e2, e3, ma2, sa2, x1, mb, c2, cc, zb, za, za2, y0, ta, tb, tc, zt;
  float w128m, w128f, w4m, w4f, e1m, e1f, e2m, e2f, e3m, e3f, lr, ph, pe, t1, a1, t2, a2, zu, zv, kf;
  bool p1, glo; int gl0, kdb, sts;
  u32x4 gt[2]; u32 kq[32], wab, g_c, g_d, g_e, nc0, nc1; float g_a, g_b, na0, nb0, na1, nb1;
  u32x4 ka[8], kb[8]; f32x4 kacc; u32 kaa, kab, kob, ko1; float kv[8];
  u64 fp[3]; u32x4 two; KParams kp;

  DEV int lane() { u32 x = l16; PIN(x); return x >> 4; }
  const Params* pp; int dch, two_off, cidx; u64 epoch; u32 dskb, cpf; u32x4 tw; float dsk;
  DEV void early() {
    const u32 po = dch * (NM * 2) + (l16 >> 2), dv = dch * 2, zero = 0;
    asm volatile("s_nop 4\n\tglobal_load_ushort %0, %8, %9\n\tglobal_load_dword %1, %7, %10\n\tglobal_load_dword %2, %7, %11\n\tglobal_load_dword %3, %7, %12\n\t"
                 "global_load_dword %4, %7, %13\n\tglobal_load_dword %5, %7, %14\n\tglobal_load_dword %6, %7, %15"
                 : "=&v"(kp.ldt), "=&v"(kp.are), "=&v"(kp.aim), "=&v"(kp.bre), "=&v"(kp.bim), "=&v"(kp.cre), "=&v"(kp.cim)
                 : "v"(po), "v"(dv), "s"(pp->logdt), "s"(pp->are), "s"(pp->aim), "s"(pp->bre), "s"(pp->bim), "s"(pp->cre), "s"(pp->cim) : "memory");
    const u32 wo = two_off + (l16 >> 8) * (D * 2) + (l16 & 240);
    asm volatile("s_nop 4\n\tglobal_load_dwordx2 %0, %3, %6 sc1\n\tglobal_load_ushort %1, %4, %7\n\tglobal_load_dwordx4 %2, %5, %8"
                 : "=&v"(epoch), "=&v"(dskb), "=&v"(tw) : "v"(zero), "v"(dv), "v"(wo), "s"(wsp), "s"(pp->dskip), "s"(pp->wd) : "memory");
  }
  DEV void early_done() {
    asm volatile("" : "+v"(kp.are), "+v"(kp.aim), "+v"(kp.bre), "+v"(kp.bim), "+v"(kp.cre), "+v"(kp.cim), "+v"(kp.ldt), "+v"(epoch), "+v"(dskb), "+v"(tw) :: "memory");
    tag = (((u64)__builtin_amdgcn_readfirstlane((u32)(epoch >> 32)) << 32) | (u32)__builtin_amdgcn_readfirstlane((u32)epoch)) + 1;
    dsk = bf2f(__builtin_amdgcn_readfirstlane(dskb));
  }
  DEV void t_write() { lds_w128(lds, TW_OFF + (4 * w + (l16 >> 8)) * 272 + (l16 & 240), tw); }
  DEV void t_read() {
    const int l = lane(), q = l >> 4, a4 = (l >> 2) & 3, p4 = l & 3;
    const u32 aw = TW_OFF + (4 * q + a4) * 272 + (64 * (w >> 2) + 32 * ((w >> 1) & 1) + 8 * p4 + 4 * (w & 1)) * 2;
    two = cat(lds_tr(lds, aw), lds_tr(lds, aw + 16 * 272));
  }
  DEV void t_store() { ws_st128s(ws, l16, tws, two); }
  template <int K> DEV int fpo() { if constexpr (K < 2) return WS_FA + K * 256 + ((l16 >> 1) & 248); else return f2s + (l16 < 880 ? l16 : 880) / 2; }
  template <int K> DEV void polls() { fp[K] = 0; if (w == 7) poll_async(fp[K], wsp, fpo<K>()); }
  template <int K> DEV void pcheck() { PIN(fp[K]); wait_flags(wsp, fpo<K>(), fp[K], tag); }
  DEV int f5c() { const int l = lane(); return h5s + (l < 13 ? l : 13) * 8; }
  float dt, lrr[2], cr[2], ci[2], gl, mx, us;
  template <int K> DEV void p0() {
    const float L2E = 1.44269504088896341f, L2E_LO = 1.92596299e-08f;
    const bool mh = (w >> 1) & 1;
    if constexpr (K == 0) {
      float x = bf2f(kp.ldt), t = x * L2E, e = __builtin_fmaf(x, L2E, -t) + x * L2E_LO;
      float rr = __builtin_amdgcn_exp2f(t);
      dt = rbf(__builtin_fmaf(rr, e * 0.69314718056f, rr)); PIN(dt);
    } else if constexpr (K == 1 || K == 2) {
      constexpr int h = K - 1;
      const float br = fmul(dt, h ? bfhi(kp.bre) : bflo(kp.bre)), bi = fmul(dt, h ? bfhi(kp.bim) : bflo(kp.bim));
      const float cre = h ? bfhi(kp.cre) : bflo(kp.cre), cim = h ? bfhi(kp.cim) : bflo(kp.cim);
      cr[h] = fnma(cim, bi, fmul(cre, br)); ci[h] = fnma(cre, bi, -fmul(cim, br));
      PIN(cr[h]); PIN(ci[h]);
    } else if constexpr (K == 3) {
      lrr[0] = fmuls(fmul(dt, bflo(kp.are)), L2E); lrr[1] = fmuls(fmul(dt, bfhi(kp.are)), L2E);
      gl = fmaxf(fmaxf(lrr[0], lrr[1]), 0.f);
      mx = fmaxf(fmaxf(fabsf(cr[0]), fabsf(ci[0])), fmaxf(fabsf(cr[1]), fabsf(ci[1])));
      PIN(gl); PIN(mx);
    } else if constexpr (K >= 4 && K < 8) {
      constexpr int CT[4] = {0xB1, 0x4E, 0x141, 0x140};
      u32 a = __float_as_uint(gl), b = __float_as_uint(mx);
      a = max(a, (u32)__builtin_amdgcn_update_dpp(0, (int)a, CT[K - 4], 0xf, 0xf, true));
      b = max(b, (u32)__builtin_amdgcn_update_dpp(0, (int)b, CT[K - 4], 0xf, 0xf, true));
      gl = __uint_as_float(a); mx = __uint_as_float(b); PIN(gl); PIN(mx);
    } else if constexpr (K == 8) {
      lr = mh ? lrr[1] : lrr[0];
      const float ai = fmul(dt, mh ? bfhi(kp.aim) : bflo(kp.aim));
      const float IH = 0.15915494309189535f, IL = 6.4206382e-09f;
      ph = fmuls(ai, IH); pe = ffmas(ai, IL, ffmas(ai, IH, -ph));
      PIN(lr); PIN(ph); PIN(pe);
      p1 = w & 1;
      t1 = p1 ? -2.f : -1.f; a1 = -t1; t2 = p1 ? -256.f : 8.f; a2 = p1 ? 256.f : 8.f;
    } else {
      const int gi = __float_as_int(gl), mi = __float_as_int(mx);
      const u32 g4 = max(max((u32)__builtin_amdgcn_readlane(gi, 0), (u32)__builtin_amdgcn_readlane(gi, 16)), max((u32)__builtin_amdgcn_readlane(gi, 32), (u32)__builtin_amdgcn_readlane(gi, 48)));
      const u32 m4 = max(max((u32)__builtin_amdgcn_readlane(mi, 0), (u32)__builtin_amdgcn_readlane(mi, 16)), max((u32)__builtin_amdgcn_readlane(mi, 32), (u32)__builtin_amdgcn_readlane(mi, 48)));
      int gx = 0, gy = 0;
      if (g4) {
        const float g = __uint_as_float(g4);
        gx = __builtin_amdgcn_readfirstlane((int)ceilf(fminf(g * 2048.f, 200.f))); gy = __builtin_amdgcn_readfirstlane((int)ceilf(fminf(g * 120.f, 100.f)));
      }
      int ex = 15 - ((int)((m4 >> 23) & 255) - 125) - gx;
      ex = ex < -100 ? -100 : ex > 100 ? 100 : ex;
      const int ey = 15 - gy;
      const float sx = __uint_as_float((u32)(127 + ex) << 23);
      kf = __uint_as_float((u32)(127 - 7 - ey) << 23);
      us = __uint_as_float((u32)(127 + 7 - ex) << 23);
      cc.r = fmuls(mh ? cr[1] : cr[0], sx); cc.i = fmuls(mh ? ci[1] : ci[0], sx); PIN(cc.r); PIN(cc.i);
      y0.r = __uint_as_float((u32)(127 + ey) << 23); y0.i = 0.f;
    }
  }
  DEV void k_addr() { wab = KG_OFF + (w >> 2) * KG_CH + (w & 1) * KG_T + ((w >> 1) & 1) * 256 + (l16 >> 2); }
  template <int K> DEV void k_put() { if (K < 30 || !(w & 1)) lds_w32(lds, wab + (K & 15) * KG_P + (K >> 4) * 2 * KG_T, kq[K]); }
  DEV void km_addr() {
    const int l = lane(), r16 = l & 15, v = w & 3;
    kab = KG_OFF + (w >> 2) * KG_CH + ((l >> 4) << 4);
    kaa = kab + (v < 2 ? v * KG_T + r16 * KG_P : 3 * KG_T + (r16 < 13 ? r16 : 13) * KG_P);
    kab += 2 * KG_T + r16 * KG_P;
    kacc = (f32x4){0.f, 0.f, 0.f, 0.f};
    const int q = l >> 4;
    int base, o1;
    if (v == 0) { base = 128 * (q >> 1) + 4 * (q & 1) + 8 * r16; o1 = 1; }
    else if (v == 1) { base = (q & 1) ? 384 + (q >> 1) + 2 * r16 : 256 + 32 * q + 4 * r16; o1 = (q & 1) ? 32 : 1; }
    else { base = 512 + 64 * q + r16; o1 = 16; }
    kob = kdb + base * 4; ko1 = o1 * 4; PIN(kob); PIN(ko1); PIN(kaa); PIN(kab);
  }
  template <int S> DEV void km_rd() { ka[S] = lds_r128(lds, kaa + S * 64); kb[S] = lds_r128(lds, kab + S * 64); }
  template <int S> DEV void km_mma() { kacc = mfma_h(ka[S], kb[S], kacc); }
  DEV void km_out() {
#pragma unroll
    for (int r = 0; r < 4; r++) lds_w32(lds, kob + r * ko1, __float_as_uint(kacc[r] * kf));
  }
  DEV void g_ld() { const u32x4* t = (const u32x4*)(g_tab.v + (w & 3) * 512) + (l16 >> 3); gt[0] = t[0]; gt[1] = t[1]; }
  DEV void kg_rd() {
#pragma unroll
    for (int i = 0; i < 8; i++) kv[i] = lds_rf(lds, gt[i >> 2][i & 3] + kdb);
  }
  DEV void kg_out() {
    u32x4 o = {pkh(kv[0], kv[1]), pkh(kv[2], kv[3]), pkh(kv[4], kv[5]), pkh(kv[6], kv[7])};
    const u32 b = KR_OFF + (w >> 2) * KR_CH + l16;
    lds_w128(lds, b + (w & 3) * 1024, o);
    if ((w & 3) == 0) { u32x4 z = {0, 0, 0, 0}; lds_w128(lds, b + 4096, z); }
  }
  template <int K> DEV void ld_conv() {
    if constexpr (K == 0) e0 = ws_ld128s(ws, l16, xs); else e1x = __builtin_amdgcn_raw_buffer_load_b64(ws, l16 + 16, xs, SC1);
  }
  template <int K> DEV void ld_epi() {
    const int l = lane();
    if constexpr (K < 2) csg[K] = __builtin_bit_cast(f32x4, ws_ld128s(ws, (l >> 4) * 16 + K * 64, cs));
    else if constexpr (K < 4) csu[K - 2] = __builtin_bit_cast(f32x4, ws_ld128s(ws, (l >> 4) * 16 + 512 + (K - 2) * 64, cs));
    else stt[K - 4] = __builtin_amdgcn_raw_buffer_load_b64(ws, (l & 15) * 8 + (K - 4) * 128, es, SC1);
  }
  template <int K> DEV void wd() { wdf[K] = ws_ld128s(ws, l16, wds + K * 1024); }
  DEV u32 ub() { return U_OFF + (w >> 2) * U_CH + (w & 3) * 1024 + 32 + l16; }
  template <int K> DEV void put() {
    const u32 n[6] = {e0[0], e0[1], e0[2], e0[3], e1x[0], e1x[1]};
    u32x4 v;
    if constexpr (K & 1) v = (u32x4){n[(K + 1) / 2], n[(K + 1) / 2 + 1], n[(K + 1) / 2 + 2], n[(K + 1) / 2 + 3]};
    else v = (u32x4){__builtin_amdgcn_alignbit(n[K / 2 + 1], n[K / 2], 16), __builtin_amdgcn_alignbit(n[K / 2 + 2], n[K / 2 + 1], 16),
                     __builtin_amdgcn_alignbit(n[K / 2 + 3], n[K / 2 + 2], 16), __builtin_amdgcn_alignbit(n[K / 2 + 4], n[K / 2 + 3], 16)};
    lds_w128(lds, ub() + K * U_PITCH, v);
  }
  DEV void put_head() {
    if ((w & 3) == 0) {
      const u32 n0 = __builtin_amdgcn_readfirstlane(e0[0]), n1 = __builtin_amdgcn_readfirstlane(e0[1]);
      const int l = lane(), k = l >> 1;
      u32x4 v = {0, 0, k == 2 ? n0 << 16 : k == 3 ? n0 : 0u, k == 0 ? n0 << 16 : k == 1 ? n0 : k == 2 ? (n0 >> 16) | (n1 << 16) : n1};
      if (!(l & 1)) v = (u32x4){0, 0, 0, 0};
      if (l < 8) lds_w128(lds, U_OFF + (w >> 2) * U_CH + k * U_PITCH + (l & 1) * 16, v);
    }
  }
  template <int C, int I> DEV void at() {
    if constexpr (C == 0 && I == 0) { early(); }
    if constexpr (C == 0 && I == 3) { g1_piece<4>(st, 2, 2 * SLOT); }
    if constexpr (C == 0 && I == 5) { g1_piece<0>(st, 2, 2 * SLOT); }
    if constexpr (C == 0 && I == 8) { g1_piece<2>(st, 2, 2 * SLOT); }
    if constexpr (C == 0 && I == 10) { g1_piece<5>(st, 2, 2 * SLOT); }
    if constexpr (C == 0 && I == 12) { g1_piece<1>(st, 2, 2 * SLOT); }
    if constexpr (C == 0 && I == 14) { g1_piece<3>(st, 2, 2 * SLOT); }
    if constexpr (C == 1 && I == 2) { g1_piece<4>(st, 3, 0 * SLOT); }
    if constexpr (C == 1 && I == 7) { g1_piece<0>(st, 3, 0 * SLOT); }
    if constexpr (C == 1 && I == 12) { g1_piece<2>(st, 3, 0 * SLOT); }
    if constexpr (C == 2 && I == 2) { g1_piece<5>(st, 3, 0 * SLOT); }
    if constexpr (C == 2 && I == 7) { g1_piece<1>(st, 3, 0 * SLOT); }
    if constexpr (C == 2 && I == 12) { g1_piece<3>(st, 3, 0 * SLOT); }
    if constexpr (C == 3 && I == 0) { early_done(); PIN(cpf); t_write(); }
    if constexpr (C == 3 && I == 2) { g1_piece<4>(st, 4, 1 * SLOT); }
    if constexpr (C == 3 && I == 3) { p0<0>(); }
    if constexpr (C == 3 && I == 7) { g1_piece<0>(st, 4, 1 * SLOT); }
    if constexpr (C == 3 && I == 8) { p0<1>(); }
    if constexpr (C == 3 && I == 12) { g1_piece<2>(st, 4, 1 * SLOT); }
    if constexpr (C == 3 && I == 14) { p0<2>(); }
    if constexpr (C == 4 && I == 2) { g1_piece<5>(st, 4, 1 * SLOT); }
    if constexpr (C == 4 && I == 4) { p0<3>(); }
    if constexpr (C == 4 && I == 7) { g1_piece<1>(st, 4, 1 * SLOT); }
    if constexpr (C == 4 && I == 9) { p0<4>(); }
    if constexpr (C == 4 && I == 11) { p0<5>(); }
    if constexpr (C == 4 && I == 12) { g1_piece<3>(st, 4, 1 * SLOT); }
    if constexpr (C == 4 && I == 13) { p0<6>(); }
    if constexpr (C == 4 && I == 15) { p0<7>(); }
    if constexpr (C == 5 && I == 0) { t_read(); }
    if constexpr (C == 5 && I == 1) { p0<8>(); }
    if constexpr (C == 5 && I == 2) { g1_piece<4>(st, 5, 2 * SLOT); }
    if constexpr (C == 5 && I == 4) { t_store(); }
    if constexpr (C == 5 && I == 5) { p0<9>(); }
    if constexpr (C == 5 && I == 7) { g1_piece<0>(st, 5, 2 * SLOT); }
    if constexpr (C == 5 && I == 12) { w128m = fmuls(lr, 128.f); w4m = fmuls(lr, 4.f); g1_piece<2>(st, 5, 2 * SLOT); }
    if constexpr (C == 5 && I == 13) { e1m = fmuls(lr, a1); w128f = fmuls(ph, -128.f); }
    if constexpr (C == 5 && I == 14) { w4f = fmuls(ph, -4.f); e1f = fmuls(ph, t1); }
    if constexpr (C == 5 && I == 15) { w128m = __builtin_amdgcn_exp2f(w128m); }
    if constexpr (C == 6 && I == 0) { w4m = __builtin_amdgcn_exp2f(w4m); }
    if constexpr (C == 6 && I == 1) { e1m = __builtin_amdgcn_exp2f(e1m); }
    if constexpr (C == 6 && I == 2) { w128f = __builtin_amdgcn_fractf(w128f); w4f = __builtin_amdgcn_fractf(w4f); g1_piece<5>(st, 5, 2 * SLOT); }
    if constexpr (C == 6 && I == 3) { e1f = __builtin_amdgcn_fractf(e1f); w128f = ffmas(pe, -128.f, w128f); }
    if constexpr (C == 6 && I == 4) { w4f = ffmas(pe, -4.f, w4f); e1f = ffmas(pe, t1, e1f); }
    if constexpr (C == 6 && I == 5) { w128.r = __builtin_amdgcn_cosf(w128f); }
    if constexpr (C == 6 && I == 6) { w4.r = __builtin_amdgcn_cosf(w4f); }
    if constexpr (C == 6 && I == 7) { e1.r = __builtin_amdgcn_cosf(e1f); g1_piece<1>(st, 5, 2 * SLOT); }
    if constexpr (C == 6 && I == 8) { w128.i = __builtin_amdgcn_sinf(w128f); }
    if constexpr (C == 6 && I == 9) { w4.i = __builtin_amdgcn_sinf(w4f); }
    if constexpr (C == 6 && I == 10) { e1.i = __builtin_amdgcn_sinf(e1f); }
    if constexpr (C == 6 && I == 11) { w128.r = fmul(w128.r, w128m); w4.r = fmul(w4.r, w4m); }
    if constexpr (C == 6 && I == 12) { e1.r = fmul(e1.r, e1m); w128.i = fmul(w128.i, w128m); g1_piece<3>(st, 5, 2 * SLOT); }
    if constexpr (C == 6 && I == 13) { w4.i = fmul(w4.i, w4m); e1.i = fmul(e1.i, e1m); }
    if constexpr (C == 6 && I == 14) { e2m = fmuls(lr, a2); e3m = fmuls(lr, 1024.f); }
    if constexpr (C == 6 && I == 15) { ma2.r = p1 ? w128.r : e1.r; PIN(ma2.r); e2f = fmuls(ph, t2); }
    if constexpr (C == 7 && I == 0) { e3f = fmuls(ph, -1024.f); ma2.i = p1 ? w128.i : e1.i; PIN(ma2.i); }
    if constexpr (C == 7 && I == 1) { e2m = __builtin_amdgcn_exp2f(e2m); }
    if constexpr (C == 7 && I == 2) { e3m = __builtin_amdgcn_exp2f(e3m); g1_piece<4>(st, 6, 0 * SLOT); }
    if constexpr (C == 7 && I == 3) { sa2.r = p1 ? w4.r : w128.r; PIN(sa2.r); e2f = __builtin_amdgcn_fractf(e2f); }
    if constexpr (C == 7 && I == 4) { e3f = __builtin_amdgcn_fractf(e3f); sa2.i = p1 ? w4.i : w128.i; PIN(sa2.i); }
    if constexpr (C == 7 && I == 5) { e2f = ffmas(pe, t2, e2f); e3f = ffmas(pe, -1024.f, e3f); }
    if constexpr (C == 7 && I == 6) { e2.r = __builtin_amdgcn_cosf(e2f); }
    if constexpr (C == 7 && I == 7) { e3.r = __builtin_amdgcn_cosf(e3f); g1_piece<0>(st, 6, 0 * SLOT); }
    if constexpr (C == 7 && I == 8) { e2.i = __builtin_amdgcn_sinf(e2f); }
    if constexpr (C == 7 && I == 9) { e3.i = __builtin_amdgcn_sinf(e3f); }
    if constexpr (C == 7 && I == 10) { e2.r = fmul(e2.r, e2m); e3.r = fmul(e3.r, e3m); }
    if constexpr (C == 7 && I == 11) { e2.i = fmul(e2.i, e2m); e3.i = fmul(e3.i, e3m); }
    if constexpr (C == 7 && I == 12) { x1.r = p1 ? e2.r : w4.r; PIN(x1.r); mb.r = p1 ? w128.r : e2.r; PIN(mb.r); g1_piece<2>(st, 6, 0 * SLOT); }
    if constexpr (C == 7 && I == 13) { x1.i = p1 ? e2.i : w4.i; PIN(x1.i); mb.i = p1 ? w128.i : e2.i; PIN(mb.i); }
    if constexpr (C == 7 && I == 14) { ta.r = fmul(cc.r, x1.r); tb.r = fmul(cc.r, e3.r); }
    if constexpr (C == 7 && I == 15) { ta.i = fmul(cc.r, x1.i); tb.i = fmul(cc.r, e3.i); }
    if constexpr (C == 8 && I == 0) { c2.r = fnma(cc.i, x1.i, ta.r); zb.r = fnma(cc.i, e3.i, tb.r); }
    if constexpr (C == 8 && I == 1) { c2.i = ffma(cc.i, x1.r, ta.i); zb.i = ffma(cc.i, e3.r, tb.i); }
    if constexpr (C == 8 && I == 2) { ta.r = fmul(c2.r, e2.r); za.r = p1 ? c2.r : cc.r; PIN(za.r); g1_piece<5>(st, 6, 0 * SLOT); }
    if constexpr (C == 8 && I == 3) { ta.i = fmul(c2.r, e2.i); za.i = p1 ? c2.i : cc.i; PIN(za.i); }
    if constexpr (C == 8 && I == 4) { za2.r = fnma(c2.i, e2.i, ta.r); zb.r = p1 ? zb.r : y0.r; PIN(zb.r); }
    if constexpr (C == 8 && I == 5) { za2.i = ffma(c2.i, e2.r, ta.i); zb.i = p1 ? zb.i : y0.i; PIN(zb.i); }
    if constexpr (C == 8 && I == 6) { za2.r = p1 ? za2.r : c2.r; PIN(za2.r); za2.i = p1 ? za2.i : c2.i; PIN(za2.i); }
    if constexpr (C == 8 && I == 7) { kq[0] = pkh(za.r, za.i); PIN(kq[0]); kq[16] = pkh(zb.r, zb.i); PIN(kq[16]); g1_piece<1>(st, 6, 0 * SLOT); }
    if constexpr (C == 8 && I == 8) { ta.r = fmul(za.r, w128.r); tb.r = fmul(zb.r, mb.r); }
    if constexpr (C == 8 && I == 9) { ta.i = fmul(za.r, w128.i); tb.i = fmul(zb.r, mb.i); }
    if constexpr (C == 8 && I == 10) { zt.r = fnma(za.i, w128.i, ta.r); zv = fnma(zb.i, mb.i, tb.r); }
    if constexpr (C == 8 && I == 11) { zt.i = ffma(za.i, w128.r, ta.i); zb.i = ffma(zb.i, mb.r, tb.i); zb.r = zv; }
    if constexpr (C == 8 && I == 12) { tc.r = fmul(za.r, e1.r); kq[17] = pkh(zb.r, zb.i); PIN(kq[17]); g1_piece<3>(st, 6, 0 * SLOT); }
    if constexpr (C == 8 && I == 13) { tc.i = fmul(za.r, e1.i); tb.r = fmul(zb.r, mb.r); }
    if constexpr (C == 8 && I == 14) { kq[8] = pkh(zt.r, zt.i); PIN(kq[8]); tb.i = fmul(zb.r, mb.i); }
    if constexpr (C == 8 && I == 15) { zu = fnma(za.i, e1.i, tc.r); zv = fnma(zb.i, mb.i, tb.r); }
    if constexpr (C == 9 && I == 0) { za.i = ffma(za.i, e1.r, tc.i); za.r = zu; zb.i = ffma(zb.i, mb.r, tb.i); zb.r = zv; }
    if constexpr (C == 9 && I == 1) { kq[1] = pkh(za.r, za.i); PIN(kq[1]); kq[18] = pkh(zb.r, zb.i); PIN(kq[18]); }
    if constexpr (C == 9 && I == 2) { ta.r = fmul(za.r, w128.r); tb.r = fmul(zb.r, mb.r); g1_piece<4>(st, 7, 1 * SLOT); }
    if constexpr (C == 9 && I == 3) { ta.i = fmul(za.r, w128.i); tb.i = fmul(zb.r, mb.i); }
    if constexpr (C == 9 && I == 4) { zt.r = fnma(za.i, w128.i, ta.r); zv = fnma(zb.i, mb.i, tb.r); }
    if constexpr (C == 9 && I == 5) { zt.i = ffma(za.i, w128.r, ta.i); zb.i = ffma(zb.i, mb.r, tb.i); zb.r = zv; }
    if constexpr (C == 9 && I == 6) { tc.r = fmul(za.r, e1.r); kq[19] = pkh(zb.r, zb.i); PIN(kq[19]); }
    if constexpr (C == 9 && I == 7) { tc.i = fmul(za.r, e1.i); tb.r = fmul(zb.r, mb.r); g1_piece<0>(st, 7, 1 * SLOT); }
    if constexpr (C == 9 && I == 8) { kq[9] = pkh(zt.r, zt.i); PIN(kq[9]); tb.i = fmul(zb.r, mb.i); }
    if constexpr (C == 9 && I == 9) { zu = fnma(za.i, e1.i, tc.r); zv = fnma(zb.i, mb.i, tb.r); }
    if constexpr (C == 9 && I == 10) { za.i = ffma(za.i, e1.r, tc.i); za.r = zu; zb.i = ffma(zb.i, mb.r, tb.i); zb.r = zv; }
    if constexpr (C == 9 && I == 11) { kq[2] = pkh(za.r, za.i); PIN(kq[2]); kq[20] = pkh(zb.r, zb.i); PIN(kq[20]); }
    if constexpr (C == 9 && I == 12) { ta.r = fmul(za.r, w128.r); tb.r = fmul(zb.r, mb.r); g1_piece<2>(st, 7, 1 * SLOT); }
    if constexpr (C == 9 && I == 13) { ta.i = fmul(za.r, w128.i); tb.i = fmul(zb.r, mb.i); }
    if constexpr (C == 9 && I == 14) { zt.r = fnma(za.i, w128.i, ta.r); zv = fnma(zb.i, mb.i, tb.r); polls<2>(); }
    if constexpr (C == 9 && I == 15) { zt.i = ffma(za.i, w128.r, ta.i); zb.i = ffma(zb.i, mb.r, tb.i); zb.r = zv; }
    if constexpr (C == 10 && I == 0) { tc.r = fmul(za.r, e1.r); kq[21] = pkh(zb.r, zb.i); PIN(kq[21]); }
    if constexpr (C == 10 && I == 1) { tc.i = fmul(za.r, e1.i); tb.r = fmul(zb.r, mb.r); }
    if constexpr (C == 10 && I == 2) { kq[10] = pkh(zt.r, zt.i); PIN(kq[10]); tb.i = fmul(zb.r, mb.i); g1_piece<5>(st, 7, 1 * SLOT); }
    if constexpr (C == 10 && I == 3) { zu = fnma(za.i, e1.i, tc.r); zv = fnma(zb.i, mb.i, tb.r); }
    if constexpr (C == 10 && I == 4) { za.i = ffma(za.i, e1.r, tc.i); za.r = zu; zb.i = ffma(zb.i, mb.r, tb.i); zb.r = zv; }
    if constexpr (C == 10 && I == 5) { kq[3] = pkh(za.r, za.i); PIN(kq[3]); kq[22] = pkh(zb.r, zb.i); PIN(kq[22]); }
    if constexpr (C == 10 && I == 6) { ta.r = fmul(za.r, w128.r); tb.r = fmul(zb.r, mb.r); }
    if constexpr (C == 10 && I == 7) { ta.i = fmul(za.r, w128.i); tb.i = fmul(zb.r, mb.i); g1_piece<1>(st, 7, 1 * SLOT); }
    if constexpr (C == 10 && I == 8) { zt.r = fnma(za.i, w128.i, ta.r); zv = fnma(zb.i, mb.i, tb.r); }
    if constexpr (C == 10 && I == 9) { zt.i = ffma(za.i, w128.r, ta.i); zb.i = ffma(zb.i, mb.r, tb.i); zb.r = zv; }
    if constexpr (C == 10 && I == 10) { kq[23] = pkh(zb.r, zb.i); PIN(kq[23]); kq[11] = pkh(zt.r, zt.i); PIN(kq[11]); }
    if constexpr (C == 10 && I == 11) { tb.r = fmul(zb.r, mb.r); kq[4] = pkh(za2.r, za2.i); PIN(kq[4]); }
    if constexpr (C == 10 && I == 12) { tb.i = fmul(zb.r, mb.i); ta.r = fmul(za2.r, sa2.r); g1_piece<3>(st, 7, 1 * SLOT); }
    if constexpr (C == 10 && I == 13) { zv = fnma(zb.i, mb.i, tb.r); ta.i = fmul(za2.r, sa2.i); }
    if constexpr (C == 10 && I == 14) { zb.i = ffma(zb.i, mb.r, tb.i); zb.r = zv; zt.r = fnma(za2.i, sa2.i, ta.r); }
    if constexpr (C == 10 && I == 15) { kq[24] = pkh(zb.r, zb.i); PIN(kq[24]); zt.i = ffma(za2.i, sa2.r, ta.i); }
    if constexpr (C == 11 && I == 0) { tb.r = fmul(zb.r, mb.r); tc.r = fmul(za2.r, ma2.r); }
    if constexpr (C == 11 && I == 1) { tb.i = fmul(zb.r, mb.i); tc.i = fmul(za2.r, ma2.i); }
    if constexpr (C == 11 && I == 2) { zv = fnma(zb.i, mb.i, tb.r); kq[12] = pkh(zt.r, zt.i); PIN(kq[12]); }
    if constexpr (C == 11 && I == 3) { zb.i = ffma(zb.i, mb.r, tb.i); zb.r = zv; zu = fnma(za2.i, ma2.i, tc.r); }
    if constexpr (C == 11 && I == 4) { kq[25] = pkh(zb.r, zb.i); PIN(kq[25]); za2.i = ffma(za2.i, ma2.r, tc.i); za2.r = zu; }
    if constexpr (C == 11 && I == 5) { tb.r = fmul(zb.r, mb.r); kq[5] = pkh(za2.r, za2.i); PIN(kq[5]); }
    if constexpr (C == 11 && I == 6) { tb.i = fmul(zb.r, mb.i); ta.r = fmul(za2.r, sa2.r); }
    if constexpr (C == 11 && I == 7) { zv = fnma(zb.i, mb.i, tb.r); ta.i = fmul(za2.r, sa2.i); }
    if constexpr (C == 11 && I == 8) { zb.i = ffma(zb.i, mb.r, tb.i); zb.r = zv; zt.r = fnma(za2.i, sa2.i, ta.r); polls<0>(); }
    if constexpr (C == 11 && I == 9) { kq[26] = pkh(zb.r, zb.i); PIN(kq[26]); zt.i = ffma(za2.i, sa2.r, ta.i); }
    if constexpr (C == 11 && I == 10) { tb.r = fmul(zb.r, mb.r); tc.r = fmul(za2.r, ma2.r); }
    if constexpr (C == 11 && I == 11) { tb.i = fmul(zb.r, mb.i); tc.i = fmul(za2.r, ma2.i); }
    if constexpr (C == 11 && I == 12) { zv = fnma(zb.i, mb.i, tb.r); kq[13] = pkh(zt.r, zt.i); PIN(kq[13]); }
    if constexpr (C == 11 && I == 13) { zb.i = ffma(zb.i, mb.r, tb.i); zb.r = zv; zu = fnma(za2.i, ma2.i, tc.r); }
    if constexpr (C == 11 && I == 14) { kq[27] = pkh(zb.r, zb.i); PIN(kq[27]); za2.i = ffma(za2.i, ma2.r, tc.i); za2.r = zu; }
    if constexpr (C == 11 && I == 15) { tb.r = fmul(zb.r, mb.r); kq[6] = pkh(za2.r, za2.i); PIN(kq[6]); }
    if constexpr (C == 12 && I == 0) { tb.i = fmul(zb.r, mb.i); ta.r = fmul(za2.r, sa2.r); }
    if constexpr (C == 12 && I == 1) { zv = fnma(zb.i, mb.i, tb.r); ta.i = fmul(za2.r, sa2.i); }
    if constexpr (C == 12 && I == 2) { zb.i = ffma(zb.i, mb.r, tb.i); zb.r = zv; zt.r = fnma(za2.i, sa2.i, ta.r); }
    if constexpr (C == 12 && I == 3) { kq[28] = pkh(zb.r, zb.i); PIN(kq[28]); zt.i = ffma(za2.i, sa2.r, ta.i); }
    if constexpr (C == 12 && I == 4) { tb.r = fmul(zb.r, mb.r); tc.r = fmul(za2.r, ma2.r); }
    if constexpr (C == 12 && I == 5) { tb.i = fmul(zb.r, mb.i); tc.i = fmul(za2.r, ma2.i); }
    if constexpr (C == 12 && I == 6) { zv = fnma(zb.i, mb.i, tb.r); kq[14] = pkh(zt.r, zt.i); PIN(kq[14]); }
    if constexpr (C == 12 && I == 7) { zb.i = ffma(zb.i, mb.r, tb.i); zb.r = zv; zu = fnma(za2.i, ma2.i, tc.r); }
    if constexpr (C == 12 && I == 8) { kq[29] = pkh(zb.r, zb.i); PIN(kq[29]); za2.i = ffma(za2.i, ma2.r, tc.i); za2.r = zu; }
    if constexpr (C == 12 && I == 9) { tb.r = fmul(zb.r, mb.r); kq[7] = pkh(za2.r, za2.i); PIN(kq[7]); }
    if constexpr (C == 12 && I == 10) { tb.i = fmul(zb.r, mb.i); ta.r = fmul(za2.r, sa2.r); }
    if constexpr (C == 12 && I == 11) { zv = fnma(zb.i, mb.i, tb.r); ta.i = fmul(za2.r, sa2.i); }
    if constexpr (C == 12 && I == 12) { zb.i = ffma(zb.i, mb.r, tb.i); zb.r = zv; zt.r = fnma(za2.i, sa2.i, ta.r); }
    if constexpr (C == 12 && I == 13) { kq[30] = pkh(zb.r, zb.i); PIN(kq[30]); zt.i = ffma(za2.i, sa2.r, ta.i); }
    if constexpr (C == 12 && I == 14) { tb.r = fmul(zb.r, mb.r); tb.i = fmul(zb.r, mb.i); }
    if constexpr (C == 12 && I == 15) { kq[15] = pkh(zt.r, zt.i); PIN(kq[15]); zv = fnma(zb.i, mb.i, tb.r); }
    if constexpr (C == 13 && I == 0) { zb.i = ffma(zb.i, mb.r, tb.i); zb.r = zv; kq[31] = pkh(zb.r, zb.i); PIN(kq[31]); k_put<0>(); }
    if constexpr (C == 13 && I == 1) { k_put<16>(); ld_epi<4>(); }
    if constexpr (C == 13 && I == 2) { k_put<17>(); }
    if constexpr (C == 13 && I == 3) { k_put<8>(); }
    if constexpr (C == 13 && I == 4) { k_put<1>(); ld_epi<5>(); }
    if constexpr (C == 13 && I == 5) { k_put<18>(); }
    if constexpr (C == 13 && I == 6) { k_put<19>(); }
    if constexpr (C == 13 && I == 7) { k_put<9>(); ld_epi<6>(); }
    if constexpr (C == 13 && I == 8) { k_put<2>(); }
    if constexpr (C == 13 && I == 9) { k_put<20>(); }
    if constexpr (C == 13 && I == 10) { k_put<21>(); ld_epi<7>(); }
    if constexpr (C == 13 && I == 11) { k_put<10>(); }
    if constexpr (C == 13 && I == 12) { k_put<3>(); ld_conv<0>(); }
    if constexpr (C == 13 && I == 13) { k_put<22>(); }
    if constexpr (C == 13 && I == 14) { k_put<23>(); ld_conv<1>(); }
    if constexpr (C == 13 && I == 15) { polls<1>(); k_put<11>(); }
    if constexpr (C == 14 && I == 0) { k_put<4>(); }
    if constexpr (C == 14 && I == 1) { k_put<24>(); }
    if constexpr (C == 14 && I == 2) { k_put<12>(); wd<0>(); }
    if constexpr (C == 14 && I == 3) { k_put<25>(); }
    if constexpr (C == 14 && I == 4) { k_put<5>(); }
    if constexpr (C == 14 && I == 5) { k_put<26>(); wd<1>(); }
    if constexpr (C == 14 && I == 6) { k_put<13>(); }
    if constexpr (C == 14 && I == 7) { k_put<27>(); }
    if constexpr (C == 14 && I == 8) { k_put<6>(); km_addr(); }
    if constexpr (C == 14 && I == 9) { k_put<28>(); wd<2>(); }
    if constexpr (C == 14 && I == 10) { k_put<14>(); }
    if constexpr (C == 14 && I == 11) { k_put<29>(); }
    if constexpr (C == 14 && I == 12) { k_put<7>(); wd<3>(); }
    if constexpr (C == 14 && I == 13) { k_put<30>(); }
    if constexpr (C == 14 && I == 14) { k_put<15>(); }
    if constexpr (C == 14 && I == 15) { k_put<31>(); }
    if constexpr (C == 15 && I == 0) { wd<4>(); ld_epi<0>(); }
    if constexpr (C == 15 && I == 1) { ld_epi<1>(); }
    if constexpr (C == 15 && I == 2) { ld_epi<2>(); }
    if constexpr (C == 15 && I == 3) { ld_epi<3>(); }
    if constexpr (C == 15 && I == 4) { wd<5>(); km_rd<0>(); }
    if constexpr (C == 15 && I == 5) { g_ld(); km_rd<1>(); }
    if constexpr (C == 15 && I == 6) { km_rd<2>(); }
    if constexpr (C == 15 && I == 7) { wd<6>(); km_rd<3>(); }
    if constexpr (C == 15 && I == 8) { km_rd<4>(); }
    if constexpr (C == 15 && I == 9) { km_rd<5>(); }
    if constexpr (C == 15 && I == 10) { km_rd<6>(); }
    if constexpr (C == 15 && I == 11) { wd<7>(); km_rd<7>(); }
  }
  template <int S> DEV void g1_wait() {
    if constexpr (S == 0) { if (w == 7) WAITV(17); else WAITV(16); }
    if constexpr (S == 1) WAITV(6);
    if constexpr (S == 2) WAITV(6);
    if constexpr (S == 3) WAITV(5);
    if constexpr (S == 4) { if (w == 7) WAITV(7); else WAITV(6); }
    if constexpr (S == 5) { if (w == 7) WAITV(3); else WAITV(6); }
    if constexpr (S == 6) WAITV(0);
    if constexpr (S == 7) { if (w == 7) WAITV(4); else WAITV(63); }
  }
  DEV void seq_p1() {
    w128m = fmuls(lr, 128.f);
    w4m = fmuls(lr, 4.f);
    e1m = fmuls(lr, a1);
    w128f = fmuls(ph, -128.f);
    w4f = fmuls(ph, -4.f);
    e1f = fmuls(ph, t1);
    w128m = __builtin_amdgcn_exp2f(w128m);
    w4m = __builtin_amdgcn_exp2f(w4m);
    e1m = __builtin_amdgcn_exp2f(e1m);
    w128f = __builtin_amdgcn_fractf(w128f);
    w4f = __builtin_amdgcn_fractf(w4f);
    e1f = __builtin_amdgcn_fractf(e1f);
    w128f = ffmas(pe, -128.f, w128f);
    w4f = ffmas(pe, -4.f, w4f);
    e1f = ffmas(pe, t1, e1f);
    w128.r = __builtin_amdgcn_cosf(w128f);
    w4.r = __builtin_amdgcn_cosf(w4f);
    e1.r = __builtin_amdgcn_cosf(e1f);
    w128.i = __builtin_amdgcn_sinf(w128f);
    w4.i = __builtin_amdgcn_sinf(w4f);
    e1.i = __builtin_amdgcn_sinf(e1f);
    w128.r = fmul(w128.r, w128m);
    w4.r = fmul(w4.r, w4m);
    e1.r = fmul(e1.r, e1m);
    w128.i = fmul(w128.i, w128m);
    w4.i = fmul(w4.i, w4m);
    e1.i = fmul(e1.i, e1m);
    e2m = fmuls(lr, a2);
    e3m = fmuls(lr, 1024.f);
    ma2.r = p1 ? w128.r : e1.r; PIN(ma2.r);
    e2f = fmuls(ph, t2);
    e3f = fmuls(ph, -1024.f);
    ma2.i = p1 ? w128.i : e1.i; PIN(ma2.i);
    e2m = __builtin_amdgcn_exp2f(e2m);
    e3m = __builtin_amdgcn_exp2f(e3m);
    sa2.r = p1 ? w4.r : w128.r; PIN(sa2.r);
    e2f = __builtin_amdgcn_fractf(e2f);
    e3f = __builtin_amdgcn_fractf(e3f);
    sa2.i = p1 ? w4.i : w128.i; PIN(sa2.i);
    e2f = ffmas(pe, t2, e2f);
    e3f = ffmas(pe, -1024.f, e3f);
    e2.r = __builtin_amdgcn_cosf(e2f);
    e3.r = __builtin_amdgcn_cosf(e3f);
    e2.i = __builtin_amdgcn_sinf(e2f);
    e3.i = __builtin_amdgcn_sinf(e3f);
    e2.r = fmul(e2.r, e2m);
    e3.r = fmul(e3.r, e3m);
    e2.i = fmul(e2.i, e2m);
    e3.i = fmul(e3.i, e3m);
    x1.r = p1 ? e2.r : w4.r; PIN(x1.r);
    mb.r = p1 ? w128.r : e2.r; PIN(mb.r);
    x1.i = p1 ? e2.i : w4.i; PIN(x1.i);
    mb.i = p1 ? w128.i : e2.i; PIN(mb.i);
    ta.r = fmul(cc.r, x1.r);
    tb.r = fmul(cc.r, e3.r);
    ta.i = fmul(cc.r, x1.i);
    tb.i = fmul(cc.r, e3.i);
    c2.r = fnma(cc.i, x1.i, ta.r);
    zb.r = fnma(cc.i, e3.i, tb.r);
    c2.i = ffma(cc.i, x1.r, ta.i);
    zb.i = ffma(cc.i, e3.r, tb.i);
    ta.r = fmul(c2.r, e2.r);
    za.r = p1 ? c2.r : cc.r; PIN(za.r);
    ta.i = fmul(c2.r, e2.i);
    za.i = p1 ? c2.i : cc.i; PIN(za.i);
    za2.r = fnma(c2.i, e2.i, ta.r);
    zb.r = p1 ? zb.r : y0.r; PIN(zb.r);
    za2.i = ffma(c2.i, e2.r, ta.i);
    zb.i = p1 ? zb.i : y0.i; PIN(zb.i);
    za2.r = p1 ? za2.r : c2.r; PIN(za2.r);
    za2.i = p1 ? za2.i : c2.i; PIN(za2.i);
  }
  DEV void seq_p2() {
    kq[0] = pkh(za.r, za.i); PIN(kq[0]);
    kq[16] = pkh(zb.r, zb.i); PIN(kq[16]);
    ta.r = fmul(za.r, w128.r);
    tb.r = fmul(zb.r, mb.r);
    ta.i = fmul(za.r, w128.i);
    tb.i = fmul(zb.r, mb.i);
    zt.r = fnma(za.i, w128.i, ta.r);
    zv = fnma(zb.i, mb.i, tb.r);
    zt.i = ffma(za.i, w128.r, ta.i);
    zb.i = ffma(zb.i, mb.r, tb.i); zb.r = zv;
    tc.r = fmul(za.r, e1.r);
    kq[17] = pkh(zb.r, zb.i); PIN(kq[17]);
    tc.i = fmul(za.r, e1.i);
    tb.r = fmul(zb.r, mb.r);
    kq[8] = pkh(zt.r, zt.i); PIN(kq[8]);
    tb.i = fmul(zb.r, mb.i);
    zu = fnma(za.i, e1.i, tc.r);
    zv = fnma(zb.i, mb.i, tb.r);
    za.i = ffma(za.i, e1.r, tc.i); za.r = zu;
    zb.i = ffma(zb.i, mb.r, tb.i); zb.r = zv;
    kq[1] = pkh(za.r, za.i); PIN(kq[1]);
    kq[18] = pkh(zb.r, zb.i); PIN(kq[18]);
    ta.r = fmul(za.r, w128.r);
    tb.r = fmul(zb.r, mb.r);
    ta.i = fmul(za.r, w128.i);
    tb.i = fmul(zb.r, mb.i);
    zt.r = fnma(za.i, w128.i, ta.r);
    zv = fnma(zb.i, mb.i, tb.r);
    zt.i = ffma(za.i, w128.r, ta.i);
    zb.i = ffma(zb.i, mb.r, tb.i); zb.r = zv;
    tc.r = fmul(za.r, e1.r);
    kq[19] = pkh(zb.r, zb.i); PIN(kq[19]);
    tc.i = fmul(za.r, e1.i);
    tb.r = fmul(zb.r, mb.r);
    kq[9] = pkh(zt.r, zt.i); PIN(kq[9]);
    tb.i = fmul(zb.r, mb.i);
    zu = fnma(za.i, e1.i, tc.r);
    zv = fnma(zb.i, mb.i, tb.r);
    za.i = ffma(za.i, e1.r, tc.i); za.r = zu;
    zb.i = ffma(zb.i, mb.r, tb.i); zb.r = zv;
    kq[2] = pkh(za.r, za.i); PIN(kq[2]);
    kq[20] = pkh(zb.r, zb.i); PIN(kq[20]);
    ta.r = fmul(za.r, w128.r);
    tb.r = fmul(zb.r, mb.r);
    ta.i = fmul(za.r, w128.i);
    tb.i = fmul(zb.r, mb.i);
    zt.r = fnma(za.i, w128.i, ta.r);
    zv = fnma(zb.i, mb.i, tb.r);
    zt.i = ffma(za.i, w128.r, ta.i);
    zb.i = ffma(zb.i, mb.r, tb.i); zb.r = zv;
    tc.r = fmul(za.r, e1.r);
    kq[21] = pkh(zb.r, zb.i); PIN(kq[21]);
    tc.i = fmul(za.r, e1.i);
    tb.r = fmul(zb.r, mb.r);
    kq[10] = pkh(zt.r, zt.i); PIN(kq[10]);
    tb.i = fmul(zb.r, mb.i);
    zu = fnma(za.i, e1.i, tc.r);
    zv = fnma(zb.i, mb.i, tb.r);
    za.i = ffma(za.i, e1.r, tc.i); za.r = zu;
    zb.i = ffma(zb.i, mb.r, tb.i); zb.r = zv;
    kq[3] = pkh(za.r, za.i); PIN(kq[3]);
    kq[22] = pkh(zb.r, zb.i); PIN(kq[22]);
    ta.r = fmul(za.r, w128.r);
    tb.r = fmul(zb.r, mb.r);
    ta.i = fmul(za.r, w128.i);
    tb.i = fmul(zb.r, mb.i);
    zt.r = fnma(za.i, w128.i, ta.r);
    zv = fnma(zb.i, mb.i, tb.r);
    zt.i = ffma(za.i, w128.r, ta.i);
    zb.i = ffma(zb.i, mb.r, tb.i); zb.r = zv;
    kq[23] = pkh(zb.r, zb.i); PIN(kq[23]);
    kq[11] = pkh(zt.r, zt.i); PIN(kq[11]);
    tb.r = fmul(zb.r, mb.r);
    kq[4] = pkh(za2.r, za2.i); PIN(kq[4]);
    tb.i = fmul(zb.r, mb.i);
    ta.r = fmul(za2.r, sa2.r);
    zv = fnma(zb.i, mb.i, tb.r);
    ta.i = fmul(za2.r, sa2.i);
    zb.i = ffma(zb.i, mb.r, tb.i); zb.r = zv;
    zt.r = fnma(za2.i, sa2.i, ta.r);
    kq[24] = pkh(zb.r, zb.i); PIN(kq[24]);
    zt.i = ffma(za2.i, sa2.r, ta.i);
    tb.r = fmul(zb.r, mb.r);
    tc.r = fmul(za2.r, ma2.r);
    tb.i = fmul(zb.r, mb.i);
    tc.i = fmul(za2.r, ma2.i);
    zv = fnma(zb.i, mb.i, tb.r);
    kq[12] = pkh(zt.r, zt.i); PIN(kq[12]);
    zb.i = ffma(zb.i, mb.r, tb.i); zb.r = zv;
    zu = fnma(za2.i, ma2.i, tc.r);
    kq[25] = pkh(zb.r, zb.i); PIN(kq[25]);
    za2.i = ffma(za2.i, ma2.r, tc.i); za2.r = zu;
    tb.r = fmul(zb.r, mb.r);
    kq[5] = pkh(za2.r, za2.i); PIN(kq[5]);
    tb.i = fmul(zb.r, mb.i);
    ta.r = fmul(za2.r, sa2.r);
    zv = fnma(zb.i, mb.i, tb.r);
    ta.i = fmul(za2.r, sa2.i);
    zb.i = ffma(zb.i, mb.r, tb.i); zb.r = zv;
    zt.r = fnma(za2.i, sa2.i, ta.r);
    kq[26] = pkh(zb.r, zb.i); PIN(kq[26]);
    zt.i = ffma(za2.i, sa2.r, ta.i);
    tb.r = fmul(zb.r, mb.r);
    tc.r = fmul(za2.r, ma2.r);
    tb.i = fmul(zb.r, mb.i);
    tc.i = fmul(za2.r, ma2.i);
    zv = fnma(zb.i, mb.i, tb.r);
    kq[13] = pkh(zt.r, zt.i); PIN(kq[13]);
    zb.i = ffma(zb.i, mb.r, tb.i); zb.r = zv;
    zu = fnma(za2.i, ma2.i, tc.r);
    kq[27] = pkh(zb.r, zb.i); PIN(kq[27]);
    za2.i = ffma(za2.i, ma2.r, tc.i); za2.r = zu;
    tb.r = fmul(zb.r, mb.r);
    kq[6] = pkh(za2.r, za2.i); PIN(kq[6]);
    tb.i = fmul(zb.r, mb.i);
    ta.r = fmul(za2.r, sa2.r);
    zv = fnma(zb.i, mb.i, tb.r);
    ta.i = fmul(za2.r, sa2.i);
    zb.i = ffma(zb.i, mb.r, tb.i); zb.r = zv;
    zt.r = fnma(za2.i, sa2.i, ta.r);
    kq[28] = pkh(zb.r, zb.i); PIN(kq[28]);
    zt.i = ffma(za2.i, sa2.r, ta.i);
    tb.r = fmul(zb.r, mb.r);
    tc.r = fmul(za2.r, ma2.r);
    tb.i = fmul(zb.r, mb.i);
    tc.i = fmul(za2.r, ma2.i);
    zv = fnma(zb.i, mb.i, tb.r);
    kq[14] = pkh(zt.r, zt.i); PIN(kq[14]);
    zb.i = ffma(zb.i, mb.r, tb.i); zb.r = zv;
    zu = fnma(za2.i, ma2.i, tc.r);
    kq[29] = pkh(zb.r, zb.i); PIN(kq[29]);
    za2.i = ffma(za2.i, ma2.r, tc.i); za2.r = zu;
    tb.r = fmul(zb.r, mb.r);
    kq[7] = pkh(za2.r, za2.i); PIN(kq[7]);
    tb.i = fmul(zb.r, mb.i);
    ta.r = fmul(za2.r, sa2.r);
    zv = fnma(zb.i, mb.i, tb.r);
    ta.i = fmul(za2.r, sa2.i);
    zb.i = ffma(zb.i, mb.r, tb.i); zb.r = zv;
    zt.r = fnma(za2.i, sa2.i, ta.r);
    kq[30] = pkh(zb.r, zb.i); PIN(kq[30]);
    zt.i = ffma(za2.i, sa2.r, ta.i);
    tb.r = fmul(zb.r, mb.r);
    tb.i = fmul(zb.r, mb.i);
    kq[15] = pkh(zt.r, zt.i); PIN(kq[15]);
    zv = fnma(zb.i, mb.i, tb.r);
    zb.i = ffma(zb.i, mb.r, tb.i); zb.r = zv;
    kq[31] = pkh(zb.r, zb.i); PIN(kq[31]);
  }

  template <int E> DEV void elem() {
    constexpr int i = E >> 1, t = E & 1;
    const float m = __uint_as_float(stt[i][0]), rd = __uint_as_float(stt[i][1]), rg = rd * -1.44269504088896341f;
    const f32x4 g = (acc[t][i] - m * csg[t]) * rg, u = (acc[2 + t][i] - m * csu[t]) * rd;
    f32x4 e;
#pragma unroll
    for (int k = 0; k < 4; k++) e[k] = __builtin_amdgcn_exp2f(g[k]);
    e += 1.f;
#pragma unroll
    for (int k = 0; k < 4; k++) e[k] = __builtin_amdgcn_rcpf(e[k]);
    e *= u;
    hq[2 * t] = pkbf(e[0], e[1]); hq[2 * t + 1] = pkbf(e[2], e[3]);
    if constexpr (t == 1) ws_st128s(ws, l16, hs + i * 56 * 1024, hq);
  }
  template <int N> DEV void publish() {
    asm volatile("s_waitcnt vmcnt(%0)" :: "i"(N) : "memory");
    BAR();
    if (w == 7 && !helper && l16 == 0) set_flag(wsp, f5o, tag);
  }
  DEV void poll() { fv = 0; if (w == 7) fv = get_flag(wsp, f5c()); }
  DEV void check() {
    if (w == 7) { const u32 ok = __all(fv == tag); __builtin_amdgcn_fence(__ATOMIC_ACQUIRE, "wavefront"); lds_w32(lds, RDY_OFF, ok); }
#pragma unroll
    for (int i = 0; i < 28; i++) PIN(wdf[i]);
    BAR();
    ready = __builtin_amdgcn_readfirstlane(lds_r32(lds, RDY_OFF));
  }
  template <int K> DEV void hl1() { glds16<SC1>(l16, hbase + (size_t)((((K + (w >> 1)) & 3) * 56 + 2 * (K >> 2)) * 1024), hslot(K >> 2) + hdst + (K & 3) * 1024); }
  template <int K> DEV void hl() { if (ready) hl1<K>(); }
};
template <int K, int N, class Fn> DEV void static_for(Fn&& f) { if constexpr (K < N) { f(std::integral_constant<int, K>{}); static_for<K + 1, N>(f); } }

template <class H> DEV void conv_mma(f32x4 (&acc)[4], LAS char* lds, u32 au, u32 au2, u32 ak, H& M) {
  u32x4 A0_0 = cat(lds_r64(lds, au + 0), lds_r64(lds, au2 + 0));
  u32x4 A0_1 = cat(lds_r64(lds, au + 512), lds_r64(lds, au2 + 512));
  M.template publish<2>();
  u32x4 B0_7 = lds_r128(lds, ak + 0);
  u32x4 B0_5 = lds_r128(lds, ak + 1024);
  u32x4 B0_3 = lds_r128(lds, ak + 2048);
  u32x4 B0_1 = lds_r128(lds, ak + 3072);
  u32x4 B0_6 = lds_r128(lds, ak + 512);
  u32x4 B0_4 = lds_r128(lds, ak + 1536);
  u32x4 B0_2 = lds_r128(lds, ak + 2560);
  SB();
  mma_h<true>(acc[0], A0_0, B0_7);
  u32x4 B0_0 = lds_r128(lds, ak + 3584);
  M.template wd<18>();
  SB();
  mma_h<true>(acc[1], A0_0, B0_5);
  u32x4 A0_2 = cat(lds_r64(lds, au + 1024), lds_r64(lds, au2 + 1024));
  SB();
  mma_h<true>(acc[2], A0_0, B0_3);
  M.template wd<19>();
  SB();
  mma_h<true>(acc[3], A0_0, B0_1);
  SB();
  mma_h<false>(acc[0], A0_1, B0_6);
  u32x4 A0_3 = cat(lds_r64(lds, au + 1536), lds_r64(lds, au2 + 1536));
  SB();
  mma_h<false>(acc[1], A0_1, B0_4);
  M.template wd<20>();
  SB();
  mma_h<false>(acc[2], A0_1, B0_2);
  SB();
  mma_h<false>(acc[3], A0_1, B0_0);
  u32x4 A0_4 = cat(lds_r64(lds, au + 2048), lds_r64(lds, au2 + 2048));
  SB();
  mma_h<false>(acc[0], A0_2, B0_5);
  u32x4 A0_5 = cat(lds_r64(lds, au + 2560), lds_r64(lds, au2 + 2560));
  M.template wd<21>();
  SB();
  mma_h<false>(acc[1], A0_2, B0_3);
  u32x4 A0_6 = cat(lds_r64(lds, au + 3072), lds_r64(lds, au2 + 3072));
  SB();
  mma_h<false>(acc[2], A0_2, B0_1);
  u32x4 A0_7 = cat(lds_r64(lds, au + 3584), lds_r64(lds, au2 + 3584));
  SB();
  mma_h<false>(acc[0], A0_3, B0_4);
  u32x4 A1_0 = cat(lds_r64(lds, au + 64), lds_r64(lds, au2 + 64));
  M.template wd<22>();
  SB();
  mma_h<false>(acc[1], A0_3, B0_2);
  u32x4 B1_7 = lds_r128(lds, ak + 64);
  SB();
  mma_h<false>(acc[2], A0_3, B0_0);
  u32x4 B1_5 = lds_r128(lds, ak + 1088);
  SB();
  mma_h<false>(acc[0], A0_4, B0_3);
  u32x4 B1_3 = lds_r128(lds, ak + 2112);
  M.template wd<23>();
  SB();
  mma_h<false>(acc[1], A0_4, B0_1);
  u32x4 B1_1 = lds_r128(lds, ak + 3136);
  SB();
  mma_h<false>(acc[0], A0_5, B0_2);
  u32x4 A1_1 = cat(lds_r64(lds, au + 576), lds_r64(lds, au2 + 576));
  SB();
  mma_h<false>(acc[1], A0_5, B0_0);
  u32x4 B1_6 = lds_r128(lds, ak + 576);
  M.template wd<24>();
  SB();
  mma_h<false>(acc[0], A0_6, B0_1);
  u32x4 B1_4 = lds_r128(lds, ak + 1600);
  SB();
  mma_h<false>(acc[0], A0_7, B0_0);
  u32x4 B1_2 = lds_r128(lds, ak + 2624);
  SB();
  mma_h<false>(acc[0], A1_0, B1_7);
  u32x4 B1_0 = lds_r128(lds, ak + 3648);
  M.template wd<25>();
  SB();
  mma_h<false>(acc[1], A1_0, B1_5);
  u32x4 A1_2 = cat(lds_r64(lds, au + 1088), lds_r64(lds, au2 + 1088));
  SB();
  mma_h<false>(acc[2], A1_0, B1_3);
  SB();
  mma_h<false>(acc[3], A1_0, B1_1);
  M.template wd<26>();
  SB();
  mma_h<false>(acc[0], A1_1, B1_6);
  u32x4 A1_3 = cat(lds_r64(lds, au + 1600), lds_r64(lds, au2 + 1600));
  SB();
  mma_h<false>(acc[1], A1_1, B1_4);
  SB();
  mma_h<false>(acc[2], A1_1, B1_2);
  M.template wd<27>();
  SB();
  mma_h<false>(acc[3], A1_1, B1_0);
  u32x4 A1_4 = cat(lds_r64(lds, au + 2112), lds_r64(lds, au2 + 2112));
  SB();
  mma_h<false>(acc[0], A1_2, B1_5);
  u32x4 A1_5 = cat(lds_r64(lds, au + 2624), lds_r64(lds, au2 + 2624));
  SB();
  mma_h<false>(acc[1], A1_2, B1_3);
  u32x4 A1_6 = cat(lds_r64(lds, au + 3136), lds_r64(lds, au2 + 3136));
  SB();
  mma_h<false>(acc[2], A1_2, B1_1);
  u32x4 A1_7 = cat(lds_r64(lds, au + 3648), lds_r64(lds, au2 + 3648));
  M.poll();
  SB();
  mma_h<false>(acc[0], A1_3, B1_4);
  u32x4 A2_0 = cat(lds_r64(lds, au + 128), lds_r64(lds, au2 + 128));
  SB();
  mma_h<false>(acc[1], A1_3, B1_2);
  u32x4 B2_7 = lds_r128(lds, ak + 128);
  SB();
  mma_h<false>(acc[2], A1_3, B1_0);
  u32x4 B2_5 = lds_r128(lds, ak + 1152);
  SB();
  mma_h<false>(acc[0], A1_4, B1_3);
  u32x4 B2_3 = lds_r128(lds, ak + 2176);
  SB();
  mma_h<false>(acc[1], A1_4, B1_1);
  u32x4 B2_1 = lds_r128(lds, ak + 3200);
  SB();
  mma_h<false>(acc[0], A1_5, B1_2);
  u32x4 A2_1 = cat(lds_r64(lds, au + 640), lds_r64(lds, au2 + 640));
  SB();
  mma_h<false>(acc[1], A1_5, B1_0);
  u32x4 B2_6 = lds_r128(lds, ak + 640);
  SB();
  mma_h<false>(acc[0], A1_6, B1_1);
  u32x4 B2_4 = lds_r128(lds, ak + 1664);
  SB();
  mma_h<false>(acc[0], A1_7, B1_0);
  u32x4 B2_2 = lds_r128(lds, ak + 2688);
  SB();
  mma_h<false>(acc[0], A2_0, B2_7);
  u32x4 B2_0 = lds_r128(lds, ak + 3712);
  SB();
  mma_h<false>(acc[1], A2_0, B2_5);
  u32x4 A2_2 = cat(lds_r64(lds, au + 1152), lds_r64(lds, au2 + 1152));
  SB();
  mma_h<false>(acc[2], A2_0, B2_3);
  M.check();
  SB();
  mma_h<false>(acc[3], A2_0, B2_1);
  M.template hl<0>();
  SB();
  mma_h<false>(acc[0], A2_1, B2_6);
  u32x4 A2_3 = cat(lds_r64(lds, au + 1664), lds_r64(lds, au2 + 1664));
  SB();
  mma_h<false>(acc[1], A2_1, B2_4);
  SB();
  mma_h<false>(acc[2], A2_1, B2_2);
  M.template hl<1>();
  SB();
  mma_h<false>(acc[3], A2_1, B2_0);
  u32x4 A2_4 = cat(lds_r64(lds, au + 2176), lds_r64(lds, au2 + 2176));
  SB();
  mma_h<false>(acc[0], A2_2, B2_5);
  u32x4 A2_5 = cat(lds_r64(lds, au + 2688), lds_r64(lds, au2 + 2688));
  SB();
  mma_h<false>(acc[1], A2_2, B2_3);
  u32x4 A2_6 = cat(lds_r64(lds, au + 3200), lds_r64(lds, au2 + 3200));
  M.template hl<2>();
  SB();
  mma_h<false>(acc[2], A2_2, B2_1);
  u32x4 A2_7 = cat(lds_r64(lds, au + 3712), lds_r64(lds, au2 + 3712));
  SB();
  mma_h<false>(acc[0], A2_3, B2_4);
  u32x4 A3_0 = cat(lds_r64(lds, au + 192), lds_r64(lds, au2 + 192));
  SB();
  mma_h<false>(acc[1], A2_3, B2_2);
  u32x4 B3_7 = lds_r128(lds, ak + 192);
  M.template hl<3>();
  SB();
  mma_h<false>(acc[2], A2_3, B2_0);
  u32x4 B3_5 = lds_r128(lds, ak + 1216);
  SB();
  mma_h<false>(acc[0], A2_4, B2_3);
  u32x4 B3_3 = lds_r128(lds, ak + 2240);
  SB();
  mma_h<false>(acc[1], A2_4, B2_1);
  u32x4 B3_1 = lds_r128(lds, ak + 3264);
  SB();
  mma_h<false>(acc[0], A2_5, B2_2);
  u32x4 A3_1 = cat(lds_r64(lds, au + 704), lds_r64(lds, au2 + 704));
  M.template hl<4>();
  SB();
  mma_h<false>(acc[1], A2_5, B2_0);
  u32x4 B3_6 = lds_r128(lds, ak + 704);
  SB();
  mma_h<false>(acc[0], A2_6, B2_1);
  u32x4 B3_4 = lds_r128(lds, ak + 1728);
  SB();
  mma_h<false>(acc[0], A2_7, B2_0);
  u32x4 B3_2 = lds_r128(lds, ak + 2752);
  M.template hl<5>();
  SB();
  mma_h<false>(acc[0], A3_0, B3_7);
  u32x4 B3_0 = lds_r128(lds, ak + 3776);
  SB();
  mma_h<false>(acc[1], A3_0, B3_5);
  u32x4 A3_2 = cat(lds_r64(lds, au + 1216), lds_r64(lds, au2 + 1216));
  SB();
  mma_h<false>(acc[2], A3_0, B3_3);
  M.template hl<6>();
  SB();
  mma_h<false>(acc[3], A3_0, B3_1);
  SB();
  mma_h<false>(acc[0], A3_1, B3_6);
  u32x4 A3_3 = cat(lds_r64(lds, au + 1728), lds_r64(lds, au2 + 1728));
  SB();
  mma_h<false>(acc[1], A3_1, B3_4);
  M.template hl<7>();
  SB();
  mma_h<false>(acc[2], A3_1, B3_2);
  SB();
  mma_h<false>(acc[3], A3_1, B3_0);
  u32x4 A3_4 = cat(lds_r64(lds, au + 2240), lds_r64(lds, au2 + 2240));
  SB();
  mma_h<false>(acc[0], A3_2, B3_5);
  SB();
  mma_h<false>(acc[1], A3_2, B3_3);
  u32x4 A3_5 = cat(lds_r64(lds, au + 2752), lds_r64(lds, au2 + 2752));
  M.template hl<8>();
  SB();
  mma_h<false>(acc[2], A3_2, B3_1);
  SB();
  mma_h<false>(acc[0], A3_3, B3_4);
  u32x4 A3_6 = cat(lds_r64(lds, au + 3264), lds_r64(lds, au2 + 3264));
  SB();
  mma_h<false>(acc[1], A3_3, B3_2);
  u32x4 A3_7 = cat(lds_r64(lds, au + 3776), lds_r64(lds, au2 + 3776));
  M.template hl<9>();
  SB();
  mma_h<false>(acc[2], A3_3, B3_0);
  SB();
  mma_h<false>(acc[0], A3_4, B3_3);
  SB();
  mma_h<false>(acc[1], A3_4, B3_1);
  M.template hl<10>();
  SB();
  mma_h<false>(acc[0], A3_5, B3_2);
  SB();
  mma_h<false>(acc[1], A3_5, B3_0);
  SB();
  mma_h<false>(acc[0], A3_6, B3_1);
  SB();
  mma_h<false>(acc[0], A3_7, B3_0);
  M.template hl<11>();
  SB();
}

template <int CL, int I, class H>
DEV void g2_step(f32x4 (&acc)[4][2], const u32x4& w0, const u32x4& w1, const u32x4 (&cur)[4], u32x4 (&nxt)[4], LAS char* lds, u32 ra, H& hk) {
  mma_bf<CL == 0>(acc[I >> 1][I & 1], (I & 1) ? w1 : w0, cur[I >> 1]);
  if constexpr (CL < 13 && I < 4) nxt[I] = lds_r128(lds, ra + I * 1024);
  hk(std::integral_constant<int, CL * 8 + I>{});
  SB();
  if constexpr (I < 7) g2_step<CL, I + 1>(acc, w0, w1, cur, nxt, lds, ra, hk);
}

__global__ void __launch_bounds__(NTHR, 2) s4glu_kernel(Params p) {
  __shared__ __attribute__((aligned(16))) char smem[LDS_BYTES];
  LAS char* lds = (LAS char*)smem;
  const int bid = blockIdx.x;
  const int w = __builtin_amdgcn_readfirstlane(threadIdx.x >> 6);
  const int xcd = bid & 7, idx = bid >> 3;
  const bool helper = idx >= 28;
  const int hid = xcd * 4 + (idx - 28);
  const int r = 4 * (xcd >> 1) + idx / 7, c = 7 * (xcd & 1) + idx % 7;
  const int rt = 4 * xcd + (idx >> 3), ct = idx & 7;
  const int gw = xcd * 28 + idx, tws = gw >> 2, cq = gw & 3;
  const int d0 = 2 * bid, cch = w >> 2;
  const int kq = w >> 1, ch2 = w & 1, wm = w >> 2, wn = w & 3;
  rsrc_t ws = __builtin_amdgcn_make_buffer_rsrc(p.ws, 0, WS_END, 0x00020000);
  Mid M;
  {
    const int tid = threadIdx.x, l = tid & 63;
    const int r16 = l & 15, q = l >> 4;
    {
      u32 swz = (l >> 4) | (((w >> 1) & 1) << 2);
      M.st.vw = (l >> 4) * (F * 2) + (((l & 15) ^ (swz << 1)) << 4);
      u32 swx = 4 * (w & 1) + (l >> 4);
      M.st.vx = (l >> 3) * (D * 2) + (((l & 7) ^ swx) << 4);
      M.st.wg = (const char*)p.wg + c * 256;
      M.st.wu = (const char*)p.wu + c * 256;
      M.st.x = (const char*)p.x + (size_t)r * 128 * D * 2;
      M.st.w = w;
      M.st.rot = 2 * (xcd >> 1);
    }
    if (!helper) {
      g1_issue(M.st, 0);
      __builtin_amdgcn_s_barrier();
      g1_issue(M.st, 1);
    }
    u32 cpf = 0;
    if (w == 7 && l < CODE_KB / 4) {
      u64 pc; asm volatile("s_getpc_b64 %0" : "=s"(pc));
      asm volatile("s_nop 4\n\tglobal_load_dword %0, %1, %2" : "+v"(cpf) : "v"((l * 32 + idx) * 128), "s"(pc & ~127ull) : "memory");
    }
    M.pp = &p; M.dch = d0 + cch; M.two_off = ((32 * (tws < 56 ? tws : 55) + 4 * w) * D + 128 * cq) * 2;
    M.ws = ws; M.wsp = p.ws; M.lds = lds; M.w = w; M.helper = helper;
    M.l16 = l * 16;
    M.xs = WS_XT + (d0 + cch) * (L * 2) + (w & 3) * 1024;
    M.cs = WS_CSUM + (c * 256 + 32 * wn) * 4;
    M.es = WS_STATS + (128 * r + 64 * wm) * 8;
    M.wds = WS_WDT + (ct * 8 + w) * 28 * 1024;
    M.hs = helper ? WS_END : WS_HT + ((8 * r + 4 * wm) * 56 + 4 * c + wn) * 1024;
    M.f5o = WS_F5 + (r * 16 + c) * 8;
    M.h5s = WS_F5 + ((rt >> 1) * 16) * 8;
    M.tws = WS_WDT + (((((2 * cq + (w >> 2)) * 4 + tws / 14) * 2 + ((w >> 1) & 1)) * 14 + tws % 14) * 2 + (w & 1)) * 1024;
    M.f2s = WS_FW + ((ct >> 1) * 64) * 8;
    M.hbase = p.ws + WS_HT + (size_t)((4 * rt) * 56 + 14 * kq + ch2) * 1024;
    M.hdst = w * 4096;
    M.kdb = KD_OFF + cch * 3072;
    M.k_addr();
    M.cpf = cpf;
    if (helper) {
      M.early();
      const int t = 8 * w + (l >> 3), c8 = l & 7, a4 = (l >> 2) & 3, p4 = l & 3;
      u32x4 xr[8];
#pragma unroll
      for (int i = 0; i < 8; i++) xr[i] = *(const u32x4*)(p.x + (size_t)(64 * hid + t) * D + (c8 + 8 * i) * 8);
      SB();
      float v[64];
      float s0 = 0.f, s1 = 0.f;
#pragma unroll
      for (int i = 0; i < 8; i++)
#pragma unroll
        for (int j = 0; j < 4; j++) {
          v[8 * i + 2 * j] = bflo(xr[i][j]); v[8 * i + 2 * j + 1] = bfhi(xr[i][j]);
          s0 += v[8 * i + 2 * j]; s1 += v[8 * i + 2 * j + 1];
        }
      const float mean = sum8(s0 + s1) * (1.f / D), m = rbf(mean);
      float q0 = 0.f, q1 = 0.f;
#pragma unroll
      for (int i = 0; i < 32; i++) {
        v[2 * i] -= m; v[2 * i + 1] -= m;
        q0 += v[2 * i] * v[2 * i]; q1 += v[2 * i + 1] * v[2 * i + 1];
      }
      const float dm = mean - m;
      const float var = fmaxf(sum8(q0 + q1) * (1.f / D) - dm * dm, 0.f);
      const float den = rbf(sqrtf(rbf(rbf(var) + 1e-5f)));
      const float rd = 1.f / den;
      if (c8 == 0) { u32x2 st = {__float_as_uint(m), __float_as_uint(rd)}; ws_st64(ws, WS_STATS + (64 * hid + t) * 8, st); }
      const u32 wb = t * 1024, fs = (t & 3) | (((t >> 3) & 3) << 2);
#pragma unroll
      for (int i = 0; i < 8; i++) {
        u32x4 o;
#pragma unroll
        for (int j = 0; j < 4; j++) {
          u32 a = pkbf(v[8 * i + 2 * j], v[8 * i + 2 * j + 1]);
          u32 b = pkbf(bflo(a) * rd, bfhi(a) * rd);
          o[j] = pkh(bflo(b), bfhi(b));
        }
        lds_w128(lds, wb + (((c8 + 8 * i) ^ fs) << 4), o);
      }
      M.early_done();
      PIN(M.cpf);
      BAR();
      {
        const u32 ad = (32 * (q & 1) + 8 * p4 + a4) * 1024 + (q >> 1) * 8, f2 = a4 | (p4 << 2);
        const u32 src = (32 * (l >> 5) + 16 * ((l >> 2) & 1) + 4 * (l & 3) + ((l >> 3) & 3)) * 4;
        u32x4 tv[8];
#pragma unroll
        for (int i = 0; i < 8; i++) { const u32 a = ad + (((8 * w + i) ^ f2) << 4); tv[i] = cat(lds_tr(lds, a), lds_tr(lds, a + 4096)); }
#pragma unroll
        for (int i = 0; i < 8; i++) {
          const u32x4 o = {bperm(src, tv[i][0]), bperm(src, tv[i][1]), bperm(src, tv[i][2]), bperm(src, tv[i][3])};
          ws_st128s(ws, (l >> 3) * (L * 2) + (l & 7) * 16, WS_XT + ((64 * w + 8 * i) * L + 64 * hid) * 2, o);
        }
      }
      constexpr u32 CS_OFF = 65536;
      const int cu = (xcd >> 1) * 4 + (idx - 28);
      const int cst = 7 * (xcd & 1) + cu % 7, cmat = cu >= 7;
      const u16* wsrc = (cmat ? p.wu : p.wg) + 128 * cst + (tid & 15) * 8 + (size_t)(tid >> 4) * F;
      u32x4 cs[16];
      MEMORD();
#pragma unroll
      for (int i = 0; i < 6; i++) cs[i] = *(const u32x4*)(wsrc + (size_t)(32 * i) * F);
      MEMORD();
      WAITV(6);
      BAR();
      if (tid == 0) set_flag(p.ws, WS_FA + hid * 8, M.tag);
#pragma unroll
      for (int i = 6; i < 16; i++) cs[i] = *(const u32x4*)(wsrc + (size_t)(32 * i) * F);
      SB();
      {
        const u32 one = (r16 & 1) ? 0x3f800000u : 0x3f80u;
        u32x4 sel = {(r16 >> 1) == 0 ? one : 0u, (r16 >> 1) == 1 ? one : 0u, (r16 >> 1) == 2 ? one : 0u, (r16 >> 1) == 3 ? one : 0u};
        f32x4 a0 = {0.f, 0.f, 0.f, 0.f}, a1 = a0;
#pragma unroll
        for (int i = 0; i < 8; i++) { a0 = mfma_bf(sel, cs[2 * i], a0); a1 = mfma_bf(sel, cs[2 * i + 1], a1); }
        a0 += a1;
        if (l < 32) lds_w128(lds, CS_OFF + w * 512 + r16 * 32 + q * 16, __builtin_bit_cast(u32x4, a0));
      }
      BAR();
      if (w == 0) {
        float a = 0.f, b = 0.f;
#pragma unroll
        for (int i = 0; i < 8; i++) { a += lds_rf(lds, CS_OFF + i * 512 + l * 8); b += lds_rf(lds, CS_OFF + i * 512 + l * 8 + 4); }
        u32x2 o = {__float_as_uint(a), __float_as_uint(b)};
        if (cu < 14) ws_st64(ws, WS_CSUM + (cst * 256 + cmat * 128 + 2 * l) * 4, o);
        WAITV(0);
        if (l == 0) set_flag(p.ws, WS_FA + 256 + hid * 8, M.tag);
      }
    }
  }
  if (!helper) {
    G1Addr a3[3];
    {
      int l; FRESH_LANE(l);
      const int r16 = l & 15, q = l >> 4, a4 = (l >> 2) & 3, p4 = l & 3;
      const u32 swz = a4 | ((q & 1) << 2);
      const u32 w0 = (8 * q + a4) * 256 + (((2 * wn) ^ swz) << 5) + 8 * p4, w1 = (8 * q + a4) * 256 + (((2 * wn + 1) ^ swz) << 5) + 8 * p4;
      const u32 x0 = 32768 + (64 * wm + r16) * 128 + ((q ^ ((r16 >> 1) & 7)) << 4);
#pragma unroll
      for (int i = 0; i < 3; i++) { a3[i].w0 = w0 + i * SLOT; a3[i].w1 = w1 + i * SLOT; a3[i].x0 = x0 + i * SLOT; a3[i].x1 = (x0 ^ 64) + i * SLOT;
        PIN(a3[i].w0); PIN(a3[i].w1); PIN(a3[i].x0); PIN(a3[i].x1); }
    }
    G1Frag fa, fb;
    if (w == 7) WAITV(7); else WAITV(6);
    BAR();
    static_for<0, 12>([&](auto i) { g1_rd<i>(fa, lds, a3[0], 0); });
    SB();
#define G1_STAGE(s)                                                                                                   \
    {                                                                                                                 \
      g1_step<2 * (s), 0>(M.acc, fa, fb, lds, a3[(s) % 3], M);                                                        \
      M.g1_wait<(s)>();                                                                                               \
      if ((s) == 5 && w == 7) M.pcheck<2>();                                                                          \
      if ((s) == 6 && w == 7) M.pcheck<0>();                                                                          \
      if ((s) == 7 && w == 7) M.pcheck<1>();                                                                          \
      BAR();                                                                                                          \
      if ((s) == 3 && w == 7 && M.l16 == 0) set_flag(p.ws, WS_FW + (cq * 64 + tws) * 8, M.tag);                        \
      SB();                                                                                                           \
      g1_step<2 * (s) + 1, 0>(M.acc, fb, fa, lds, a3[((s) + 1) % 3], M);                                              \
    }
    G1_STAGE(0) G1_STAGE(1) G1_STAGE(2) G1_STAGE(3) G1_STAGE(4) G1_STAGE(5) G1_STAGE(6) G1_STAGE(7)
#undef G1_STAGE
    static_for<0, 8>([&](auto i) { M.km_mma<i>(); });
    MFMA_DONE();
  } else {
    static_for<0, 10>([&](auto i) { M.p0<i>(); });
    M.seq_p1(); M.seq_p2();
    static_for<0, 32>([&](auto i) { M.k_put<i>(); });
    if (w == 7) { M.fp[0] = ~M.tag; M.pcheck<0>(); M.fp[2] = ~M.tag; M.pcheck<2>(); }
    BAR();
    M.ld_conv<0>(); M.ld_conv<1>(); M.g_ld();
    M.km_addr();
    static_for<0, 8>([&](auto i) { M.km_rd<i>(); });
    static_for<0, 8>([&](auto i) { M.km_mma<i>(); });
    static_for<0, NWD0>([&](auto i) { M.wd<i>(); });
  }
  const u64 tag = M.tag;
  const float dsk = M.dsk;
  static_for<0, 8>([&](auto e) {
    static_for<NWE * e / 8, NWE * (e + 1) / 8>([&](auto k) { M.wd<NWD0 + k>(); });
    MEMORD();
    if (!helper) M.elem<e>();
    if constexpr (e == 0) M.km_out();
  });
  MEMORD();
  static_for<0, 4>([&](auto i) { M.put<i>(); });
  M.put_head();
  const int nset = w & 1, wh = (w >> 1) & 1;
  const int nn0 = (wh ? 3 : 7) - nset;
  u32 au, au2, ak, ua;
  {
    const int l = M.lane(), r16 = l & 15, q = l >> 4;
    au = U_OFF + cch * U_CH + (r16 & 3) * U_PITCH + (8 * q + 4 * (r16 >> 2)) * 2 + wh * 256;
    au2 = au + 8;
    ak = KR_OFF + cch * KR_CH + (2032 - 16 * r16 + 8 * q) * 2 - 512 * 7 + wh * 256 + nset * 512;
    ua = U_OFF + cch * U_CH + 3 * U_PITCH + (256 * nn0 + 16 * r16 + 4 * q - 512 + 12) * 2;
    PIN(au); PIN(au2); PIN(ak); PIN(ua);
  }
  BAR();
  M.kg_rd();
  u32 sk[4];
  {
    const u32x2 u0 = lds_r64(lds, ua + 1024), u1 = lds_r64(lds, ua);
    M.kg_out();
    static_for<0, NWP>([&](auto i) { M.wd<NWD0 + NWE + i>(); });
    sk[0] = pkbf(h2f_lo(u0[0]) * dsk, h2f_hi(u0[0]) * dsk); sk[1] = pkbf(h2f_lo(u0[1]) * dsk, h2f_hi(u0[1]) * dsk);
    sk[2] = pkbf(h2f_lo(u1[0]) * dsk, h2f_hi(u1[0]) * dsk); sk[3] = pkbf(h2f_lo(u1[1]) * dsk, h2f_hi(u1[1]) * dsk);
    PIN(sk[0]); PIN(sk[1]); PIN(sk[2]); PIN(sk[3]);
  }
  f32x4 k0, k1;
  {
    f32x4 acc[4];
    conv_mma(acc, lds, au, au2, ak, M);
    const u32 cw = cr_off(w) + M.l16;
    MFMA_DONE();
    if (wh) { lds_w128(lds, cw, __builtin_bit_cast(u32x4, acc[0])); lds_w128(lds, cw + 1024, __builtin_bit_cast(u32x4, acc[1])); k0 = acc[2]; k1 = acc[3]; }
    else { lds_w128(lds, cw, __builtin_bit_cast(u32x4, acc[2])); lds_w128(lds, cw + 1024, __builtin_bit_cast(u32x4, acc[3])); k0 = acc[0]; k1 = acc[1]; }
  }
  {
  int l; FRESH_LANE(l);
  const int tid = w * 64 + l, q = l >> 4, a4 = (l >> 2) & 3, p4 = l & 3;
  const u32 hv = l * 16, hdst = w * 4096;
  const char* hbase = M.hbase;
  if (!M.ready) {
    if (w == 7) { spin_flags(p.ws, M.f5c(), tag); __builtin_amdgcn_fence(__ATOMIC_ACQUIRE, "wavefront"); }
    BAR();
    static_for<0, 12>([&](auto i) { M.hl1<i>(); });
  }
  u32 rd0 = kq * 8192 + l * 16, rd1 = rd0 + SLOT + HS;
  PIN(rd0); PIN(rd1);
  WAITV(8);
  BAR();
  {
    u32x4 fa[4], fb[4], sv, xv;
#define RDA(j) (((j) & 3) == 3 ? rd0 + U_OFF : ((j) & 3) == 0 ? rd0 + SLOT : rd1 + (((j) & 3) - 1) * HS)
#pragma unroll
    for (int i = 0; i < 4; i++) fa[i] = lds_r128(lds, RDA(0) + i * 1024);
    const f32x4 c0 = __builtin_bit_cast(f32x4, lds_r128(lds, cr_off(w ^ 2) + hv)), c1 = __builtin_bit_cast(f32x4, lds_r128(lds, cr_off(w ^ 2) + hv + 1024));
    f32x4 acc[4][2];
    u64 fsv = 0, fev = 0;
    const int fso = WS_F6 + (32 * ct + (l & 31)) * 8, feo = WS_F6 + (tid & 255) * 8;
    const u32 src = (16 * (l & 3) + (l >> 2)) * 4;
    u32 so[4];
    auto hk = [&](auto K) {
      constexpr int k = K;
      if constexpr (k >= 1 && k < 5) {
        constexpr int i = k - 1, e = 2 * (i & 1);
        const float ya = fmuls(fadd(i < 2 ? k0[e] : k1[e], i < 2 ? c0[e] : c1[e]), M.us), yb = fmuls(fadd(i < 2 ? k0[e + 1] : k1[e + 1], i < 2 ? c0[e + 1] : c1[e + 1]), M.us);
        const u32 y = pkbf(ya, yb);
        so[i] = bperm(src, pkbf(fadd(bflo(y), bflo(sk[i])), fadd(bfhi(y), bfhi(sk[i]))));
      }
      constexpr int cl = k >> 3, st = cl == 0 ? 3 : cl == 1 ? 4 : cl == 3 ? 5 : cl == 5 ? 6 : -1;
      if constexpr (st >= 0 && (k & 1) == 0) {
        constexpr int i = (k & 7) >> 1;
        glds16<SC1>(hv, hbase + (size_t)((((i + kq) & 3) * 56 + 2 * st) * 1024), hslot(st) + hdst + i * 1024);
      }
      if constexpr (k == 7) {
        u32x2 o0 = {so[0], so[1]}, o1 = {so[2], so[3]};
        __builtin_amdgcn_raw_buffer_store_b64(o0, ws, l * 8, WS_ST + ((d0 + cch) * L + 256 * nn0) * 2, SC1);
        __builtin_amdgcn_raw_buffer_store_b64(o1, ws, l * 8, WS_ST + ((d0 + cch) * L + 256 * nn0 - 512) * 2, SC1);
      }
    };
    SB();
#define G2_STAGE(j)                                                                                                   \
    {                                                                                                                 \
      g2_step<2 * (j), 0>(acc, M.wdf[4 * (j)], M.wdf[4 * (j) + 1], fa, fb, lds, RDA(j) + 4096, hk);                   \
      if ((j) == 0) WAITV(10); else if ((j) == 1 || (j) == 4) WAITV(4); else if ((j) <= 3) WAITV(8); else if ((j) == 5) WAITV(0); \
      if ((j) == 6 && w == 7) wait_flags(p.ws, fso, fsv, tag);                                                        \
      BAR();                                                                                                          \
      if ((j) == 1 && tid == 0) set_flag(p.ws, WS_F6 + bid * 8, tag);                                                 \
      if ((j) == 5) {                                                                                                 \
        if (w == 7) fsv = get_flag(p.ws, fso);                                                                        \
        if (bid == 0) fev = get_flag(p.ws, feo);                                                                      \
        xv = *(const u32x4*)(p.x + (size_t)(64 * rt + 16 * kq + (l >> 2)) * D + 64 * ct + 32 * ch2 + 8 * (l & 3));    \
      }                                                                                                               \
      if ((j) == 6) sv = ws_ld128(ws, WS_ST + ((64 * ct + (tid >> 3)) * L + 64 * rt + (tid & 7) * 8) * 2);            \
      SB();                                                                                                           \
      g2_step<2 * (j) + 1, 0>(acc, M.wdf[4 * (j) + 2], M.wdf[4 * (j) + 3], fb, fa, lds, RDA((j) + 1), hk);            \
    }
    G2_STAGE(0) G2_STAGE(1) G2_STAGE(2) G2_STAGE(3) G2_STAGE(4) G2_STAGE(5) G2_STAGE(6)
#undef G2_STAGE
#undef RDA
    MFMA_DONE();
#pragma unroll
    for (int i = 1; i < 4; i++) {
      const u32 o = R2_OFF + (((((i + kq) & 3) * 2 + ch2) * 3 + i - 1) * 2) * 1024 + l * 16;
      lds_w128(lds, o, __builtin_bit_cast(u32x4, acc[i][0]));
      lds_w128(lds, o + 1024, __builtin_bit_cast(u32x4, acc[i][1]));
    }
    lds_w128(lds, ST_OFF + tid * 16, sv);
    f32x4 a0 = acc[0][0], a1 = acc[0][1];
    BAR();
#pragma unroll
    for (int s = 0; s < 3; s++) {
      u32 o = R2_OFF + ((w * 3 + s) * 2) * 1024 + l * 16;
      a0 += __builtin_bit_cast(f32x4, lds_r128(lds, o));
      a1 += __builtin_bit_cast(f32x4, lds_r128(lds, o + 1024));
    }
    u32 sa = ST_OFF + (32 * ch2 + 8 * q + a4) * 128 + (16 * kq + 4 * p4) * 2;
    u32x2 s0 = lds_tr(lds, sa), s1 = lds_tr(lds, sa + 512);
    u32 ab[4] = {pkbf(a0[0], a0[1]), pkbf(a0[2], a0[3]), pkbf(a1[0], a1[1]), pkbf(a1[2], a1[3])};
    u32 sw[4] = {s0[0], s0[1], s1[0], s1[1]};
    u32x4 o;
#pragma unroll
    for (int j = 0; j < 4; j++) {
      float al = bflo(ab[j]), ah = bfhi(ab[j]);
      u32 g = pkbf(al * __builtin_amdgcn_rcpf(1.f + __builtin_amdgcn_exp2f(al * -1.44269504088896341f)),
                   ah * __builtin_amdgcn_rcpf(1.f + __builtin_amdgcn_exp2f(ah * -1.44269504088896341f)));
      u32 sg = bperm(src, pkbf(bflo(sw[j]) * bflo(g), bfhi(sw[j]) * bfhi(g)));
      o[j] = pkbf(bflo(xv[j]) + bflo(sg), bfhi(xv[j]) + bfhi(sg));
    }
    __builtin_amdgcn_raw_buffer_store_b128(o, __builtin_amdgcn_make_buffer_rsrc(p.out, 0, L * D * 2, 0x00020000), ((64 * rt + 16 * kq + (l >> 2)) * D + 64 * ct + 32 * ch2 + 8 * (l & 3)) * 2, 0, SC1);
    if (bid == 0) {
      wait_flags(p.ws, feo, fev, tag);
      BAR();
      if (tid == 0) set_flag(p.ws, WS_EPOCH, tag);
    }
  }
  }
}

extern "C" void kernel_launch(void* const* d_in, const int* in_sizes, int n_in, void* d_out, int out_size, void* d_ws,
                              size_t ws_size, hipStream_t stream) {
  Params p;
  p.x = (const u16*)d_in[0]; p.are = (const u16*)d_in[1]; p.aim = (const u16*)d_in[2]; p.bre = (const u16*)d_in[3];
  p.bim = (const u16*)d_in[4]; p.cre = (const u16*)d_in[5]; p.cim = (const u16*)d_in[6]; p.logdt = (const u16*)d_in[7];
  p.dskip = (const u16*)d_in[8]; p.wg = (const u16*)d_in[10]; p.wu = (const u16*)d_in[11]; p.wd = (const u16*)d_in[12];
  p.out = (u16*)d_out; p.ws = (char*)d_ws;
  s4glu_kernel<<<256, NTHR, 0, stream>>>(p);
}
```
